```python
import torch

def _softmax(x, axis=-1):
    return torch.softmax(x, dim=axis)
def _gelu(x):
    return torch.nn.functional.gelu(x, approximate="tanh")
def _silu(x):
    return torch.nn.functional.silu(x)
def _sigmoid(x):
    return torch.sigmoid(x)
def _layernorm(x, axis=-1, eps=1e-5):
    m = x.mean(dim=axis, keepdim=True)
    v = x.var(dim=axis, keepdim=True, unbiased=False)
    return (x - m) / torch.sqrt(v + eps)
def _scatter_add(tgt, idx, src, axis=0):
    return tgt.clone().index_add_(axis, idx, src)
def _segment_sum(src, seg, n):
    out = torch.zeros((n,) + src.shape[1:], dtype=src.dtype)
    return out.index_add_(0, seg, src)
def _pad(x, pw):
    flat = tuple(v for pair in reversed(pw) for v in pair)
    return torch.nn.functional.pad(x, flat)

kernel_name = 'sequential_preLN__softmax_full_mha__swiglu_ffn__prompt'

def setup_inputs():

    _w = (lambda a: (lambda t: t.to(torch.bfloat16) if t.is_floating_point() else t)(torch.as_tensor(a)))
    def _A_setup():
        B, S, D = 1, 2048, 512
        return {
            "x": _w((lambda *s: torch.randn(*s, dtype=torch.bfloat16) * 0.05)(B, S, D)),
            "wqkv": _w((lambda *s: torch.randn(*s, dtype=torch.bfloat16) * 0.05)(D, 3 * D)),
            "wo": _w((lambda *s: torch.randn(*s, dtype=torch.bfloat16) * 0.05)(D, D)),
        }
    _A_in = _A_setup()

    def _B_setup():
        B, S, D, F = 1, 2048, 512, 1792
        return {
            "x": _w((lambda *s: torch.randn(*s, dtype=torch.bfloat16) * 0.05)(B, S, D)),
            "w_gate": _w((lambda *s: torch.randn(*s, dtype=torch.bfloat16) * 0.05)(D, F)),
            "w_up": _w((lambda *s: torch.randn(*s, dtype=torch.bfloat16) * 0.05)(D, F)),
            "w_down": _w((lambda *s: torch.randn(*s, dtype=torch.bfloat16) * 0.05)(F, D)),
        }
    _B_in = _B_setup()

    return {
        "A_x": _A_in["x"],
        "A_wqkv": _A_in["wqkv"],
        "A_wo": _A_in["wo"],
        "B_x": _B_in["x"],
        "B_w_gate": _B_in["w_gate"],
        "B_w_up": _B_in["w_up"],
        "B_w_down": _B_in["w_down"],
    }

def reference(A_x, A_wqkv, A_wo, B_x, B_w_gate, B_w_up, B_w_down):
    def _A_ref(x, wqkv, wo):
        B, S, D = x.shape
        H = 32; Dh = D // H
        qkv = (lambda x, *d: x.permute(*d))((x @ wqkv).reshape(B, S, 3, H, Dh), 2, 0, 3, 1, 4)
        q, k, v = qkv[0], qkv[1], qkv[2]
        scale = Dh ** -0.5
        scores = torch.einsum("bhqd,bhkd->bhqk", q, k) * scale
        p = _softmax(scores, axis=-1)
        o = torch.einsum("bhqk,bhkd->bhqd", p, v)
        return (lambda x, *d: x.permute(*d))(o, 0, 2, 1, 3).reshape(B, S, D) @ wo

    def _B_ref(x, w_gate, w_up, w_down):
        g = _silu(x @ w_gate)
        u = x @ w_up
        return (g * u) @ w_down

    x = A_x
    h = x + _A_ref(_layernorm(x, axis=-1), A_wqkv, A_wo)
    h2 = h + _B_ref(_layernorm(h, axis=-1), B_w_gate, B_w_up, B_w_down)
    return h2
```

```cpp
#include <hip/hip_runtime.h>
#include <hip/hip_bf16.h>
#include <stdint.h>

typedef __attribute__((ext_vector_type(2))) __bf16 bf16x2;
typedef __attribute__((ext_vector_type(8))) __bf16 bf16x8;
typedef __attribute__((ext_vector_type(16))) __bf16 bf16x16;
typedef __attribute__((ext_vector_type(4))) short s16x4;
typedef __attribute__((ext_vector_type(2))) float f32x2;
typedef __attribute__((ext_vector_type(4))) float f32x4;
typedef __attribute__((ext_vector_type(8))) float f32x8;
typedef __attribute__((ext_vector_type(16))) float f32x16;
typedef __attribute__((ext_vector_type(2))) unsigned u32x2;
typedef __attribute__((ext_vector_type(4))) unsigned u32x4;
typedef __attribute__((address_space(3))) char lds_char;
typedef unsigned short u16;

#define DEV __device__ __forceinline__
#define SEQ 2048
#define DM 512
#define FF 1792

DEV void glds16(const void* sbase, unsigned voff, unsigned ldsaddr) {
  asm volatile("s_mov_b32 m0, %2\n\ts_nop 0\n\tglobal_load_lds_dwordx4 %0, %1" ::"v"(voff), "s"(sbase), "s"(ldsaddr) : "memory");
}
template <int N> DEV void wait_vm() { asm volatile("s_waitcnt vmcnt(%0)" ::"n"(N) : "memory"); }
template <int P, int D> DEV void wait_tail(int rem) {
  asm volatile(
      "s_waitcnt vmcnt(%1)\n\ts_cmp_ge_i32 %0, %2\n\ts_cbranch_scc1 1f\n\t"
      "s_waitcnt vmcnt(%3)\n\ts_cmp_ge_i32 %0, 3\n\ts_cbranch_scc1 1f\n\t"
      "s_waitcnt vmcnt(%4)\n\ts_cmp_ge_i32 %0, 2\n\ts_cbranch_scc1 1f\n\t"
      "s_waitcnt vmcnt(%5)\n\ts_cmp_ge_i32 %0, 1\n\ts_cbranch_scc1 1f\n\t"
      "s_waitcnt vmcnt(0)\n1:" ::"s"(rem), "n"(P * D), "n"(D), "n"(P * (D < 3 ? D : 3)), "n"(P * (D < 2 ? D : 2)), "n"(P)
      : "memory", "scc");
}
DEV void bar() {
  asm volatile("s_waitcnt lgkmcnt(0)" ::: "memory");
  __builtin_amdgcn_s_barrier();
  asm volatile("" ::: "memory");
}
DEV u32x4 lds128(const lds_char* p) { return *(const __attribute__((address_space(3))) u32x4*)p; }
DEV u32x2 lds_tr(const lds_char* p) {
  s16x4 v = __builtin_amdgcn_ds_read_tr16_b64_v4i16((__attribute__((address_space(3))) s16x4*)p);
  return __builtin_bit_cast(u32x2, v);
}
DEV u32x4 lds_tr2(const lds_char* p, int second) {
  u32x2 a = lds_tr(p), b = lds_tr(p + second);
  return u32x4{a[0], a[1], b[0], b[1]};
}
DEV f32x4 mfma16(u32x4 a, u32x4 b, f32x4 c) {
  return __builtin_amdgcn_mfma_f32_16x16x32_bf16(__builtin_bit_cast(bf16x8, a), __builtin_bit_cast(bf16x8, b), c, 0, 0, 0);
}
DEV f32x16 mfma32(u32x4 a, u32x4 b, f32x16 c) {
  return __builtin_amdgcn_mfma_f32_32x32x16_bf16(__builtin_bit_cast(bf16x8, a), __builtin_bit_cast(bf16x8, b), c, 0, 0, 0);
}
DEV float dot2(unsigned a, unsigned b, float c) {
  return __builtin_amdgcn_fdot2_f32_bf16(__builtin_bit_cast(bf16x2, a), __builtin_bit_cast(bf16x2, b), c, false);
}
DEV float sum8(u32x4 v, float c) {
  c = dot2(v[0], 0x3f803f80u, c); c = dot2(v[1], 0x3f803f80u, c);
  c = dot2(v[2], 0x3f803f80u, c); c = dot2(v[3], 0x3f803f80u, c);
  return c;
}
DEV float sq8(u32x4 v, float c) {
  c = dot2(v[0], v[0], c); c = dot2(v[1], v[1], c); c = dot2(v[2], v[2], c); c = dot2(v[3], v[3], c);
  return c;
}
DEV unsigned pack2(float lo, float hi) {
  bf16x2 v = {(__bf16)lo, (__bf16)hi};
  return __builtin_bit_cast(unsigned, v);
}
DEV float u2f(unsigned u) { return __builtin_bit_cast(float, u); }
DEV unsigned f2u(float f) { return __builtin_bit_cast(unsigned, f); }
DEV float bflo(unsigned u) { return __builtin_bit_cast(float, u << 16); }
DEV float bfhi(unsigned u) { return __builtin_bit_cast(float, u & 0xffff0000u); }
DEV float xsum32(float v) {
  unsigned u = __builtin_bit_cast(unsigned, v);
  auto r = __builtin_amdgcn_permlane32_swap(u, u, false, false);
  return u2f(r[0]) + u2f(r[1]);
}
DEV float xsum16(float v) {
  unsigned u = __builtin_bit_cast(unsigned, v);
  auto r = __builtin_amdgcn_permlane16_swap(u, u, false, false);
  return u2f(r[0]) + u2f(r[1]);
}
#define LDSF(p) (*(__attribute__((address_space(3))) float*)(p))
#define LDSF2(p) (*(__attribute__((address_space(3))) f32x2*)(p))
#define LDSF4(p) (*(__attribute__((address_space(3))) f32x4*)(p))

#define K1_NS 4
struct K1F { u32x4 A0, A1, B0, B1, B2, C0, C1, D0, D1, D2; };
__global__ __launch_bounds__(512, 2) void k1_qkv(const u16* __restrict__ x, const u16* __restrict__ w,
                                                 u16* __restrict__ qimg, u16* __restrict__ kvimg) {
  __shared__ __attribute__((aligned(16))) char smem_[K1_NS * 32768 + 4096];
  lds_char* smem = (lds_char*)smem_;
  const int l = threadIdx.x & 63, wv = __builtin_amdgcn_readfirstlane(threadIdx.x >> 6);
  const int hg = blockIdx.x & 7, rb = blockIdx.x >> 3;
  const int rh = wv & 1, hd = wv >> 1;
  const int lr = wv * 8 + (l >> 3);
  const unsigned ga = lr * (DM * 2) + (((l & 7) ^ ((lr >> 1) & 7)) << 4);
  const unsigned gb = lr * (1536 * 2) + (((l & 7) ^ (2 * (((lr >> 1) & 1) | (((lr >> 3) & 1) << 1)))) << 4);
  const char* xa = (const char*)x + (size_t)rb * (64 * DM * 2);
  const char* wb = (const char*)w + hg * 128;
  const unsigned lbase = (unsigned)(uintptr_t)smem + wv * 1024;
  const int rot = rb & 7;
  auto issue = [&](int st) {
    int kt = (st + rot) & 7;
    unsigned d = lbase + (st % K1_NS) * 32768;
    glds16(xa + kt * 128, ga, d);
    const char* b = wb + (size_t)kt * (64 * 1536 * 2);
    glds16(b, gb, d + 8192);
    glds16(b + 1024, gb, d + 16384);
    glds16(b + 2048, gb, d + 24576);
  };
#pragma unroll
  for (int s = 0; s < K1_NS; ++s) issue(s);

  const int g = l >> 4, c16 = l & 15;
  const int ksf = rh ^ (hd >> 1), tf = hd & 1;
  const int arow = rh * 32 + c16;
  const int a00 = (arow + tf * 16) * 128 + (((ksf * 4 + g) ^ (c16 >> 1)) << 4);
  const int a01 = a00 ^ 2048, a10 = a00 ^ 64, a11 = a00 ^ 2048 ^ 64;
  const int q4 = c16 >> 2, p4 = l & 3;
  const int b0 = 8192 + (ksf * 32 + 8 * g + q4) * 128 + (((hd * 2 + (p4 >> 1)) ^ (2 * ((q4 >> 1) | ((g & 1) << 1)))) << 4) + (p4 & 1) * 8;
  const int b1 = b0 ^ 4096;
  f32x4 acc[3][2] = {};
  float s1 = 0.f, s2 = 0.f, cs[3] = {0.f, 0.f, 0.f};
  auto rd = [&](K1F& f, int st) {
    const lds_char* sb = smem + (st % K1_NS) * 32768;
    f.A0 = lds128(sb + a00); f.B0 = lds_tr2(sb + b0, 512); f.A1 = lds128(sb + a01);
    f.B1 = lds_tr2(sb + b0 + 8192, 512); f.B2 = lds_tr2(sb + b0 + 16384, 512);
    f.C0 = lds128(sb + a10); f.D0 = lds_tr2(sb + b1, 512); f.C1 = lds128(sb + a11);
    f.D1 = lds_tr2(sb + b1 + 8192, 512); f.D2 = lds_tr2(sb + b1 + 16384, 512);
  };
  auto mm = [&](const K1F& f) {
    acc[0][0] = mfma16(f.B0, f.A0, acc[0][0]);
    acc[0][1] = mfma16(f.B0, f.A1, acc[0][1]);
    acc[1][0] = mfma16(f.B1, f.A0, acc[1][0]);
    acc[1][1] = mfma16(f.B1, f.A1, acc[1][1]);
    acc[2][0] = mfma16(f.A0, f.B2, acc[2][0]);
    acc[2][1] = mfma16(f.A1, f.B2, acc[2][1]);
    s1 = sum8(f.A0, s1); s2 = sq8(f.A0, s2);
    cs[0] = sum8(f.B0, cs[0]); cs[1] = sum8(f.B1, cs[1]); cs[2] = sum8(f.B2, cs[2]);
    acc[0][0] = mfma16(f.D0, f.C0, acc[0][0]);
    acc[0][1] = mfma16(f.D0, f.C1, acc[0][1]);
    acc[1][0] = mfma16(f.D1, f.C0, acc[1][0]);
    acc[1][1] = mfma16(f.D1, f.C1, acc[1][1]);
    acc[2][0] = mfma16(f.C0, f.D2, acc[2][0]);
    acc[2][1] = mfma16(f.C1, f.D2, acc[2][1]);
  };
  K1F F, G;
  wait_vm<4 * (K1_NS - 1)>();
  bar();
  rd(F, 0);
#pragma unroll 1
  for (int i = 0; i < 8; i += 2) {
    wait_tail<4, K1_NS - 2>(6 - i);
    bar();
    if (i + K1_NS < 8) issue(i + K1_NS);
    rd(G, i + 1);
    __builtin_amdgcn_sched_barrier(0);
    mm(F);
    if (i < 6) {
      wait_tail<4, K1_NS - 2>(5 - i);
      bar();
      if (i + 1 + K1_NS < 8) issue(i + 1 + K1_NS);
      rd(F, i + 2);
    }
    __builtin_amdgcn_sched_barrier(0);
    mm(G);
  }
  lds_char* st = smem + K1_NS * 32768;
  s1 = xsum16(xsum32(s1)); s2 = xsum16(xsum32(s2));
  cs[0] = xsum16(xsum32(cs[0])); cs[1] = xsum16(xsum32(cs[1])); cs[2] = xsum16(xsum32(cs[2]));
  if (g == 0) LDSF2(st + ((arow + tf * 16) * 2 + (hd >> 1)) * 8) = f32x2{s1, s2};
  else LDSF(st + 1024 + (((hd * 3 + g - 1) * 2 + rh) * 16 + c16) * 4) = g == 1 ? cs[0] : (g == 2 ? cs[1] : cs[2]);
  bar();
  lds_char* fin = smem + wv * 256;
  {
    f32x4 p = LDSF4(st + (rh * 32 + (l & 31)) * 16);
    float m = (p[0] + p[2]) * (1.f / DM);
    float var = fmaxf((p[1] + p[3]) * (1.f / DM) - m * m, 0.f);
    float r = __builtin_amdgcn_rsqf(var + 1e-5f);
    if (l < 32) LDSF2(fin + l * 8) = f32x2{r, -m * r};
  }
  const lds_char* cp = st + 1024 + hd * 3 * 128;
  f32x4 cq = LDSF4(cp + g * 16) + LDSF4(cp + 64 + g * 16);
  f32x4 ck = LDSF4(cp + 128 + g * 16) + LDSF4(cp + 192 + g * 16);
  float cv = LDSF(cp + 256 + c16 * 4) + LDSF(cp + 320 + c16 * 4);
  const int head = hg * 4 + hd, t32 = rb * 2 + rh;
  char* qo = (char*)qimg + (size_t)(head * 64 + t32) * 1024 + (g >> 1) * 512 + (g & 1) * 8;
  char* ko = (char*)kvimg + (size_t)(head * 64 + t32) * 2048;
  const float qs = 0.25f * 1.44269504088896340736f;
  u32x2 vp[2];
#pragma unroll
  for (int t = 0; t < 2; ++t) {
    const int tt = t ^ tf;
    f32x2 rm = LDSF2(fin + (tt * 16 + c16) * 8);
    f32x4 qv = (acc[0][t] * rm[0] + cq * rm[1]) * qs;
    f32x4 kv = acc[1][t] * rm[0] + ck * rm[1];
    *(u32x2*)(qo + (tt * 16 + c16) * 16) = u32x2{pack2(qv[0], qv[1]), pack2(qv[2], qv[3])};
    *(u32x2*)(ko + (g >> 1) * 512 + (g & 1) * 8 + (8 * q4 + 4 * tt + p4) * 16) = u32x2{pack2(kv[0], kv[1]), pack2(kv[2], kv[3])};
    f32x4 r01 = LDSF4(fin + (tt * 16 + 4 * g) * 8), r23 = LDSF4(fin + (tt * 16 + 4 * g) * 8 + 16);
    vp[t] = u32x2{pack2(acc[2][t][0] * r01[0] + cv * r01[1], acc[2][t][1] * r01[2] + cv * r01[3]),
                  pack2(acc[2][t][2] * r23[0] + cv * r23[1], acc[2][t][3] * r23[2] + cv * r23[3])};
  }
  u32x4 vv = tf ? u32x4{vp[1][0], vp[1][1], vp[0][0], vp[0][1]} : u32x4{vp[0][0], vp[0][1], vp[1][0], vp[1][1]};
  *(u32x4*)(ko + 1024 + l * 16) = vv;
}

DEV f32x16 pv(u32x4 v, f32x8 p, f32x16 o) {
  return __builtin_amdgcn_smfmac_f32_32x32x32_bf16(__builtin_bit_cast(bf16x8, v), __builtin_bit_cast(bf16x16, p), o, 0xdddd, 0, 0);
}
__global__ __launch_bounds__(512, 2) void k2_attn(const u16* __restrict__ qimg, const u16* __restrict__ kvimg,
                                                  u16* __restrict__ og) {
  __shared__ __attribute__((aligned(16))) char smem_[131072 + 4096 + 8192];
  lds_char* smem = (lds_char*)smem_;
  const int l = threadIdx.x & 63, wv = __builtin_amdgcn_readfirstlane(threadIdx.x >> 6);
  const int head = (blockIdx.x & 7) * 4 + ((blockIdx.x >> 3) & 3), qb = blockIdx.x >> 5;
  const char* gk = (const char*)kvimg + (size_t)head * 131072 + wv * 1024;
  const unsigned lbase = (unsigned)(uintptr_t)smem + wv * 1024;
  glds16((const char*)qimg + (size_t)(head * 64 + qb * 8 + wv) * 1024, l * 16, lbase + 135168);
#pragma unroll
  for (int s = 0; s < 16; ++s) glds16(gk + s * 8192, l * 16, lbase + s * 8192);
  *(__attribute__((address_space(3))) u32x2*)(smem + 131072 + threadIdx.x * 8) = u32x2{0x3f803f80u, 0x3f803f80u};
  const bool isv = (l & 16) == 0;
  const int va0 = isv ? 1024 + (l >> 5) * 256 + (l & 15) * 16 : 131072 + (l & 15) * 16;
  const int vstep = isv ? 4096 : 0;
  f32x16 cinit = {};
  f32x16 o;
  wait_vm<14>();
  bar();
  const u32x4 q = lds128(smem + 135168 + wv * 1024 + l * 16);
#pragma unroll 1
  for (int attempt = 0; attempt < 2; ++attempt) {
    o = f32x16{};
    int ka = l * 16, va = va0;
    f32x16 s0 = mfma32(lds128(smem + ka), q, cinit);
#pragma unroll 1
    for (int t = attempt * 64; t < attempt * 64 + 64; t += 2) {
      if (t == 6) { wait_vm<11>(); bar(); }
      if (t == 18) { wait_vm<0>(); bar(); }
      u32x4 k1 = lds128(smem + ka + 2048);
      u32x4 v00 = lds128(smem + va), v01 = lds128(smem + va + 512);
      u32x4 k2 = lds128(smem + ka + 4096);
      u32x4 v10 = lds128(smem + va + 2048), v11 = lds128(smem + va + 2560);
      f32x16 s1 = mfma32(k1, q, cinit);
      f32x8 p0, p1;
#pragma unroll
      for (int i = 0; i < 8; ++i) p0[i] = __builtin_amdgcn_exp2f(s0[i]);
      o = pv(v00, p0, o);
#pragma unroll
      for (int i = 0; i < 8; ++i) p1[i] = __builtin_amdgcn_exp2f(s0[8 + i]);
      o = pv(v01, p1, o);
      s0 = mfma32(k2, q, cinit);
#pragma unroll
      for (int i = 0; i < 8; ++i) p0[i] = __builtin_amdgcn_exp2f(s1[i]);
      o = pv(v10, p0, o);
#pragma unroll
      for (int i = 0; i < 8; ++i) p1[i] = __builtin_amdgcn_exp2f(s1[8 + i]);
      o = pv(v11, p1, o);
      ka += 4096; va += vstep;
    }
    if (attempt) break;
    float am = 0.f;
#pragma unroll
    for (int i = 0; i < 8; ++i) am = fmaxf(am, fabsf(o[i]));
    bool ok = o[8] >= 0x1p-60f && o[8] <= 0x1p100f && am <= 0x1p126f;
#pragma unroll
    for (int i = 0; i < 8; ++i) ok = ok && (o[i] == o[i]);
    if (__builtin_amdgcn_ballot_w64(!ok) == 0) break;
    float mx = -__builtin_inff();
#pragma unroll 1
    for (int t = 0; t < 64; ++t) {
      f32x16 s = mfma32(lds128(smem + t * 2048 + l * 16), q, f32x16{});
#pragma unroll
      for (int i = 0; i < 16; ++i) mx = fmaxf(mx, s[i]);
    }
    unsigned u = __builtin_bit_cast(unsigned, mx);
    auto r = __builtin_amdgcn_permlane32_swap(u, u, false, false);
    mx = fmaxf(u2f(r[0]), u2f(r[1]));
#pragma unroll
    for (int i = 0; i < 16; ++i) cinit[i] = -mx;
  }
  const float inv = 1.f / o[8];
  u32x4 ov = {pack2(o[0] * inv, o[1] * inv), pack2(o[2] * inv, o[3] * inv), pack2(o[4] * inv, o[5] * inv), pack2(o[6] * inv, o[7] * inv)};
  *(u32x4*)((char*)og + ((size_t)(head * 2 + (l >> 5)) * SEQ + qb * 256 + wv * 32 + (l & 31)) * 16) = ov;
}

struct KPF { u32x4 A0, B0, B1, A1, D0, D1; };
template <int KDIM, int NS, int MODE>
__global__ __launch_bounds__(512, 2) void k_proj(const u16* __restrict__ a, const u16* __restrict__ w,
                                                 const u16* __restrict__ res, u16* __restrict__ out) {
  constexpr int NT = KDIM / 64;
  __shared__ __attribute__((aligned(16))) char smem_[NS * 16384];
  lds_char* smem = (lds_char*)smem_;
  const int l = threadIdx.x & 63, wv = __builtin_amdgcn_readfirstlane(threadIdx.x >> 6);
  const int xcd = blockIdx.x & 7, j = blockIdx.x >> 3;
  const int rb = xcd * 4 + (j & 3), cb = j >> 2;
  const int lr = wv * 8 + (l >> 3);
  const char* sa = (const char*)a + ((size_t)wv * SEQ + rb * 64) * 16;
  const char* sw = (const char*)w + cb * 128;
  const unsigned gb = lr * (DM * 2) + (((l & 7) ^ (2 * ((lr >> 1) & 3))) << 4);
  const unsigned lbase = (unsigned)(uintptr_t)smem + wv * 1024;
  int kt = (j * NT) >> 5;
  int slot = 0;
  auto issue = [&]() {
    unsigned d = lbase + slot * 16384;
    glds16(sa + (size_t)kt * (8 * SEQ * 16), l * 16, d);
    glds16(sw + (size_t)kt * (64 * DM * 2), gb, d + 8192);
    kt = kt + 1 == NT ? 0 : kt + 1;
    slot = slot + 1 == NS ? 0 : slot + 1;
  };
#pragma unroll
  for (int s = 0; s < NS; ++s) issue();
  const int g = l >> 4, c16 = l & 15;
  const int rt = wv & 3, ch = wv >> 2;
  const int tok = rb * 64 + rt * 16 + c16, col = cb * 64 + ch * 32 + (g & 1) * 16 + (g >> 1) * 8;
  const size_t off_rm = ((size_t)tok * DM + col) * 2, off_g8 = ((size_t)(col >> 3) * SEQ + tok) * 16;
  const int a0 = g * 1024 + (rt * 16 + c16) * 16;
  const int q4 = c16 >> 2, p4 = l & 3;
  const int brow = 16 * (g >> 1) + 4 * (g & 1) + q4;
  const int b0 = 8192 + brow * 128 + (((ch * 4 + (p4 >> 1)) ^ (2 * ((brow >> 1) & 3))) << 4) + (p4 & 1) * 8;
  const int b1 = b0 ^ 32;
  f32x4 acc0 = {}, acc1 = {};
  int cur = 0;
  auto rd = [&](KPF& f) {
    const lds_char* sb = smem + cur * 16384;
    cur = cur + 1 == NS ? 0 : cur + 1;
    f.A0 = lds128(sb + a0); f.B0 = lds_tr2(sb + b0, 1024); f.B1 = lds_tr2(sb + b1, 1024);
    f.A1 = lds128(sb + a0 + 4096); f.D0 = lds_tr2(sb + b0 + 4096, 1024); f.D1 = lds_tr2(sb + b1 + 4096, 1024);
  };
  auto mm = [&](const KPF& f) {
    acc0 = mfma16(f.B0, f.A0, acc0);
    acc1 = mfma16(f.B1, f.A0, acc1);
    acc0 = mfma16(f.D0, f.A1, acc0);
    acc1 = mfma16(f.D1, f.A1, acc1);
  };
  KPF F, G;
  u32x4 rv;
  wait_vm<2 * (NS - 1)>();
  bar();
  rd(F);
#pragma unroll 1
  for (int i = 0; i < NT; i += 2) {
    wait_tail<2, NS - 2>(NT - 2 - i);
    bar();
    if (i + NS < NT) issue();
    rd(G);
    __builtin_amdgcn_sched_barrier(0);
    mm(F);
    if (i < NT - 2) {
      wait_tail<2, NS - 2>(NT - 3 - i);
      bar();
      if (i + 1 + NS < NT) issue();
      rd(F);
    }
    __builtin_amdgcn_sched_barrier(0);
    mm(G);
  }
  rv = *(const u32x4*)((const char*)res + (MODE == 0 ? off_rm : off_g8));
  float v[8];
#pragma unroll
  for (int i = 0; i < 4; ++i) {
    auto r = __builtin_amdgcn_permlane16_swap(f2u(acc0[i]), f2u(acc1[i]), false, false);
    v[i] = u2f(r[0]);
    v[4 + i] = u2f(r[1]);
  }
  u32x4 ov;
#pragma unroll
  for (int i = 0; i < 4; ++i) ov[i] = pack2(v[2 * i] + bflo(rv[i]), v[2 * i + 1] + bfhi(rv[i]));
  *(u32x4*)((char*)out + (MODE == 0 ? off_g8 : off_rm)) = ov;
}

#define K4_NS 6
struct K4F { u32x4 A0, G0, A1, U0, C0, G1, C1, U1; };
__global__ __launch_bounds__(512, 2) void k4_gateup(const u16* __restrict__ h, const u16* __restrict__ wg,
                                                    const u16* __restrict__ wu, u16* __restrict__ act) {
  __shared__ __attribute__((aligned(16))) char smem_[K4_NS * 24576 + 4096];
  lds_char* smem = (lds_char*)smem_;
  const int l = threadIdx.x & 63, wv = __builtin_amdgcn_readfirstlane(threadIdx.x >> 6);
  const int xcd = blockIdx.x & 7, j = blockIdx.x >> 3;
  const int rb = (xcd >> 1) * 4 + (j & 3), cb = (xcd & 1) * 7 + (j >> 2);
  const char* sa = (const char*)h + ((size_t)(wv >> 1) * SEQ + rb * 128 + (wv & 1) * 64) * 16;
  const int lr = wv * 4 + (l >> 4);
  const unsigned gb = lr * (FF * 2) + (((l & 15) ^ (4 * (lr & 3))) << 4);
  const char* sg = (const char*)wg + cb * 256;
  const char* su = (const char*)wu + cb * 256;
  const unsigned lbase = (unsigned)(uintptr_t)smem + wv * 1024;
  int kt = (j * 16) / 28;
  int slot = 0;
  auto issue = [&]() {
    unsigned d = lbase + slot * 24576;
    glds16(sa + (size_t)kt * (4 * SEQ * 16), l * 16, d);
    glds16(sg + (size_t)kt * (32 * FF * 2), gb, d + 8192);
    glds16(su + (size_t)kt * (32 * FF * 2), gb, d + 16384);
    kt = (kt + 1) & 15;
    slot = slot + 1 == K4_NS ? 0 : slot + 1;
  };
#pragma unroll
  for (int s = 0; s < K4_NS; ++s) issue();
  const int rh = wv & 1, cg = wv >> 1;
  const int ksf = rh ^ (cg >> 1), tf = cg & 1;
  const int hb = l >> 5, r32 = l & 31;
  const int a00 = (ksf * 2 + hb) * 2048 + (rh * 64 + tf * 32 + r32) * 16;
  const int a01 = a00 ^ 512, a10 = a00 ^ 4096, a11 = a00 ^ 4096 ^ 512;
  const int q4 = (l & 15) >> 2, p4 = l & 3;
  const int b0 = 8192 + (ksf * 16 + hb * 8 + q4) * 256 + (((cg * 4 + ((l >> 4) & 1) * 2 + (p4 >> 1)) ^ (4 * q4)) << 4) + (p4 & 1) * 8;
  const int b1 = b0 ^ 4096;
  f32x16 ag0 = {}, ag1 = {}, au0 = {}, au1 = {};
  float s1 = 0.f, s2 = 0.f, csg = 0.f, csu = 0.f;
  int cur = 0;
  auto rd = [&](K4F& f) {
    const lds_char* sb = smem + cur * 24576;
    cur = cur + 1 == K4_NS ? 0 : cur + 1;
    f.A0 = lds128(sb + a00); f.G0 = lds_tr2(sb + b0, 1024); f.A1 = lds128(sb + a01); f.U0 = lds_tr2(sb + b0 + 8192, 1024);
    f.C0 = lds128(sb + a10); f.G1 = lds_tr2(sb + b1, 1024); f.C1 = lds128(sb + a11); f.U1 = lds_tr2(sb + b1 + 8192, 1024);
  };
  auto mm = [&](const K4F& f) {
    ag0 = mfma32(f.G0, f.A0, ag0);
    s1 = sum8(f.A0, s1);
    ag1 = mfma32(f.G0, f.A1, ag1);
    s2 = sq8(f.A0, s2);
    au0 = mfma32(f.U0, f.A0, au0);
    csg = sum8(f.G0, csg);
    au1 = mfma32(f.U0, f.A1, au1);
    csu = sum8(f.U0, csu);
    ag0 = mfma32(f.G1, f.C0, ag0);
    ag1 = mfma32(f.G1, f.C1, ag1);
    au0 = mfma32(f.U1, f.C0, au0);
    au1 = mfma32(f.U1, f.C1, au1);
  };
  K4F F, G;
  wait_vm<3 * (K4_NS - 1)>();
  bar();
  rd(F);
#pragma unroll 1
  for (int i = 0; i < 16; i += 2) {
    wait_tail<3, K4_NS - 2>(14 - i);
    bar();
    if (i + K4_NS < 16) issue();
    rd(G);
    __builtin_amdgcn_sched_barrier(0);
    mm(F);
    if (i < 14) {
      wait_tail<3, K4_NS - 2>(13 - i);
      bar();
      if (i + 1 + K4_NS < 16) issue();
      rd(F);
    }
    __builtin_amdgcn_sched_barrier(0);
    mm(G);
  }
  lds_char* st = smem + K4_NS * 24576;
  s1 = xsum32(s1); s2 = xsum32(s2); csg = xsum32(csg); csu = xsum32(csu);
  if (hb == 0) LDSF2(st + ((rh * 64 + tf * 32 + r32) * 2 + (cg >> 1)) * 8) = f32x2{s1, s2};
  LDSF(st + 2048 + (((cg * 2 + hb) * 2 + rh) * 32 + r32) * 4) = hb ? csu : csg;
  bar();
  const lds_char* cp = st + 2048 + cg * 512 + hb * 16;
  char* ao = (char*)act + ((size_t)(cb * 16 + cg * 4 + hb) * SEQ + rb * 128 + rh * 64 + r32) * 16;
#pragma unroll
  for (int t = 0; t < 2; ++t) {
    const int tt = t ^ tf;
    f32x4 p = LDSF4(st + (rh * 64 + tt * 32 + r32) * 16);
    float m = (p[0] + p[2]) * (1.f / DM);
    float var = fmaxf((p[1] + p[3]) * (1.f / DM) - m * m, 0.f);
    float r = __builtin_amdgcn_rsqf(var + 1e-5f);
    float mr = -m * r;
    unsigned pk[8];
#pragma unroll
    for (int c = 0; c < 4; ++c) {
      f32x4 cgv = LDSF4(cp + c * 32) + LDSF4(cp + 128 + c * 32);
      f32x4 cuv = LDSF4(cp + 256 + c * 32) + LDSF4(cp + 384 + c * 32);
      float y[4];
#pragma unroll
      for (int e = 0; e < 4; ++e) {
        float gv = (t ? ag1[c * 4 + e] : ag0[c * 4 + e]) * r + cgv[e] * mr;
        float uv = (t ? au1[c * 4 + e] : au0[c * 4 + e]) * r + cuv[e] * mr;
        float sg_ = __builtin_amdgcn_rcpf(1.f + __builtin_amdgcn_exp2f(gv * -1.44269504088896340736f));
        y[e] = gv * sg_ * uv;
      }
      pk[c * 2] = pack2(y[0], y[1]);
      pk[c * 2 + 1] = pack2(y[2], y[3]);
    }
    *(u32x4*)(ao + tt * 512) = u32x4{pk[0], pk[1], pk[2], pk[3]};
    *(u32x4*)(ao + tt * 512 + 2 * SEQ * 16) = u32x4{pk[4], pk[5], pk[6], pk[7]};
  }
}

extern "C" void kernel_launch(void* const* d_in, const int* in_sizes, int n_in, void* d_out, int out_size,
                              void* d_ws, size_t ws_size, hipStream_t stream) {
  const u16* x = (const u16*)d_in[0];
  const u16* wqkv = (const u16*)d_in[1];
  const u16* wo = (const u16*)d_in[2];
  const u16* wg = (const u16*)d_in[4];
  const u16* wu = (const u16*)d_in[5];
  const u16* wd = (const u16*)d_in[6];
  char* ws = (char*)d_ws;
  u16* qimg = (u16*)ws;
  u16* kvimg = (u16*)(ws + (2u << 20));
  u16* og = (u16*)(ws + (6u << 20));
  u16* h = (u16*)(ws + (8u << 20));
  u16* act = (u16*)(ws + (10u << 20));
  k1_qkv<<<256, 512, 0, stream>>>(x, wqkv, qimg, kvimg);
  k2_attn<<<256, 512, 0, stream>>>(qimg, kvimg, og);
  k_proj<512, 8, 0><<<256, 512, 0, stream>>>(og, wo, x, h);
  k4_gateup<<<224, 512, 0, stream>>>(h, wg, wu, act);
  k_proj<1792, 9, 1><<<256, 512, 0, stream>>>(act, wd, h, (u16*)d_out);
}
```

```cpp
#include <hip/hip_runtime.h>
#include <hip/hip_bf16.h>
#include <stdint.h>

typedef __attribute__((ext_vector_type(2))) __bf16 bf16x2;
typedef __attribute__((ext_vector_type(8))) __bf16 bf16x8;
typedef __attribute__((ext_vector_type(16))) __bf16 bf16x16;
typedef __attribute__((ext_vector_type(4))) short s16x4;
typedef __attribute__((ext_vector_type(2))) float f32x2;
typedef __attribute__((ext_vector_type(4))) float f32x4;
typedef __attribute__((ext_vector_type(8))) float f32x8;
typedef __attribute__((ext_vector_type(16))) float f32x16;
typedef __attribute__((ext_vector_type(2))) unsigned u32x2;
typedef __attribute__((ext_vector_type(4))) unsigned u32x4;
typedef __attribute__((address_space(3))) char lds_char;
typedef unsigned short u16;

#define DEV __device__ __forceinline__
#define TS(k, i)
#define SEQ 2048
#define DM 512
#define FF 1792

DEV void glds16(const void* sbase, unsigned voff, unsigned ldsaddr) {
  asm volatile("s_mov_b32 m0, %2\n\ts_nop 0\n\tglobal_load_lds_dwordx4 %0, %1" ::"v"(voff), "s"(sbase), "s"(ldsaddr) : "memory");
}
template <int N> DEV void wait_vm() { asm volatile("s_waitcnt vmcnt(%0)" ::"n"(N) : "memory"); }
template <int P, int D> DEV void wait_tail(int rem) {
  asm volatile(
      "s_waitcnt vmcnt(%1)\n\ts_cmp_ge_i32 %0, %2\n\ts_cbranch_scc1 1f\n\t"
      "s_waitcnt vmcnt(%3)\n\ts_cmp_ge_i32 %0, 3\n\ts_cbranch_scc1 1f\n\t"
      "s_waitcnt vmcnt(%4)\n\ts_cmp_ge_i32 %0, 2\n\ts_cbranch_scc1 1f\n\t"
      "s_waitcnt vmcnt(%5)\n\ts_cmp_ge_i32 %0, 1\n\ts_cbranch_scc1 1f\n\t"
      "s_waitcnt vmcnt(0)\n1:" ::"s"(rem), "n"(P * D), "n"(D), "n"(P * (D < 3 ? D : 3)), "n"(P * (D < 2 ? D : 2)), "n"(P)
      : "memory", "scc");
}
DEV void bar() {
  asm volatile("s_waitcnt lgkmcnt(0)" ::: "memory");
  __builtin_amdgcn_s_barrier();
  asm volatile("" ::: "memory");
}
DEV u32x4 lds128(const lds_char* p) { return *(const __attribute__((address_space(3))) u32x4*)p; }
DEV u32x2 lds_tr(const lds_char* p) {
  s16x4 v = __builtin_amdgcn_ds_read_tr16_b64_v4i16((__attribute__((address_space(3))) s16x4*)p);
  return __builtin_bit_cast(u32x2, v);
}
DEV u32x4 lds_tr2(const lds_char* p, int second) {
  u32x2 a = lds_tr(p), b = lds_tr(p + second);
  return u32x4{a[0], a[1], b[0], b[1]};
}
DEV f32x4 mfma16(u32x4 a, u32x4 b, f32x4 c) {
  return __builtin_amdgcn_mfma_f32_16x16x32_bf16(__builtin_bit_cast(bf16x8, a), __builtin_bit_cast(bf16x8, b), c, 0, 0, 0);
}
DEV f32x16 mfma32(u32x4 a, u32x4 b, f32x16 c) {
  return __builtin_amdgcn_mfma_f32_32x32x16_bf16(__builtin_bit_cast(bf16x8, a), __builtin_bit_cast(bf16x8, b), c, 0, 0, 0);
}
DEV float dot2(unsigned a, unsigned b, float c) {
  return __builtin_amdgcn_fdot2_f32_bf16(__builtin_bit_cast(bf16x2, a), __builtin_bit_cast(bf16x2, b), c, false);
}
DEV float sum8(u32x4 v, float c) {
  c = dot2(v[0], 0x3f803f80u, c); c = dot2(v[1], 0x3f803f80u, c);
  c = dot2(v[2], 0x3f803f80u, c); c = dot2(v[3], 0x3f803f80u, c);
  return c;
}
DEV float sq8(u32x4 v, float c) {
  c = dot2(v[0], v[0], c); c = dot2(v[1], v[1], c); c = dot2(v[2], v[2], c); c = dot2(v[3], v[3], c);
  return c;
}
DEV unsigned pack2(float lo, float hi) {
  bf16x2 v = {(__bf16)lo, (__bf16)hi};
  return __builtin_bit_cast(unsigned, v);
}
DEV float u2f(unsigned u) { return __builtin_bit_cast(float, u); }
DEV unsigned f2u(float f) { return __builtin_bit_cast(unsigned, f); }
DEV float bflo(unsigned u) { return __builtin_bit_cast(float, u << 16); }
DEV float bfhi(unsigned u) { return __builtin_bit_cast(float, u & 0xffff0000u); }
DEV float xsum32(float v) {
  unsigned u = __builtin_bit_cast(unsigned, v);
  auto r = __builtin_amdgcn_permlane32_swap(u, u, false, false);
  return u2f(r[0]) + u2f(r[1]);
}
DEV float xsum16(float v) {
  unsigned u = __builtin_bit_cast(unsigned, v);
  auto r = __builtin_amdgcn_permlane16_swap(u, u, false, false);
  return u2f(r[0]) + u2f(r[1]);
}
#define LDSF(p) (*(__attribute__((address_space(3))) float*)(p))
#define LDSF2(p) (*(__attribute__((address_space(3))) f32x2*)(p))
#define LDSF4(p) (*(__attribute__((address_space(3))) f32x4*)(p))

#define K1_NS 4
struct K1F { u32x4 A0, A1, B0, B1, B2, C0, C1, D0, D1, D2; };
__global__ __launch_bounds__(512, 2) void k1_qkv(const u16* __restrict__ x, const u16* __restrict__ w,
                                                 u16* __restrict__ qimg, u16* __restrict__ kvimg) {
  __shared__ __attribute__((aligned(16))) char smem_[K1_NS * 32768 + 4096];
  lds_char* smem = (lds_char*)smem_;
  const int l = threadIdx.x & 63, wv = __builtin_amdgcn_readfirstlane(threadIdx.x >> 6);
  TS(0, 0);
  const int hg = blockIdx.x & 7, rb = blockIdx.x >> 3;
  const int rh = wv & 1, hd = wv >> 1;
  const int lr = wv * 8 + (l >> 3);
  const unsigned ga = lr * (DM * 2) + (((l & 7) ^ ((lr >> 1) & 7)) << 4);
  const unsigned gb = lr * (1536 * 2) + (((l & 7) ^ (2 * (((lr >> 1) & 1) | (((lr >> 3) & 1) << 1)))) << 4);
  const char* xa = (const char*)x + (size_t)rb * (64 * DM * 2);
  const char* wb = (const char*)w + hg * 128;
  const unsigned lbase = (unsigned)(uintptr_t)smem + wv * 1024;
  const int rot = rb & 7;
  auto issue = [&](int st) {
    int kt = (st + rot) & 7;
    unsigned d = lbase + (st % K1_NS) * 32768;
    glds16(xa + kt * 128, ga, d);
    const char* b = wb + (size_t)kt * (64 * 1536 * 2);
    glds16(b, gb, d + 8192);
    glds16(b + 1024, gb, d + 16384);
    glds16(b + 2048, gb, d + 24576);
  };
#pragma unroll
  for (int s = 0; s < K1_NS; ++s) issue(s);

  const int g = l >> 4, c16 = l & 15;
  const int ksf = rh ^ (hd >> 1), tf = hd & 1;
  const int arow = rh * 32 + c16;
  const int a00 = (arow + tf * 16) * 128 + (((ksf * 4 + g) ^ (c16 >> 1)) << 4);
  const int a01 = a00 ^ 2048, a10 = a00 ^ 64, a11 = a00 ^ 2048 ^ 64;
  const int q4 = c16 >> 2, p4 = l & 3;
  const int b0 = 8192 + (ksf * 32 + 8 * g + q4) * 128 + (((hd * 2 + (p4 >> 1)) ^ (2 * ((q4 >> 1) | ((g & 1) << 1)))) << 4) + (p4 & 1) * 8;
  const int b1 = b0 ^ 4096;
  f32x4 acc[3][2] = {};
  float s1 = 0.f, s2 = 0.f, cs[3] = {0.f, 0.f, 0.f};
  auto rd = [&](K1F& f, int st) {
    const lds_char* sb = smem + (st % K1_NS) * 32768;
    f.A0 = lds128(sb + a00); f.B0 = lds_tr2(sb + b0, 512); f.A1 = lds128(sb + a01);
    f.B1 = lds_tr2(sb + b0 + 8192, 512); f.B2 = lds_tr2(sb + b0 + 16384, 512);
    f.C0 = lds128(sb + a10); f.D0 = lds_tr2(sb + b1, 512); f.C1 = lds128(sb + a11);
    f.D1 = lds_tr2(sb + b1 + 8192, 512); f.D2 = lds_tr2(sb + b1 + 16384, 512);
  };
  auto mm = [&](const K1F& f) {
    acc[0][0] = mfma16(f.B0, f.A0, acc[0][0]);
    acc[0][1] = mfma16(f.B0, f.A1, acc[0][1]);
    acc[1][0] = mfma16(f.B1, f.A0, acc[1][0]);
    acc[1][1] = mfma16(f.B1, f.A1, acc[1][1]);
    acc[2][0] = mfma16(f.A0, f.B2, acc[2][0]);
    acc[2][1] = mfma16(f.A1, f.B2, acc[2][1]);
    s1 = sum8(f.A0, s1); s2 = sq8(f.A0, s2);
    cs[0] = sum8(f.B0, cs[0]); cs[1] = sum8(f.B1, cs[1]); cs[2] = sum8(f.B2, cs[2]);
    acc[0][0] = mfma16(f.D0, f.C0, acc[0][0]);
    acc[0][1] = mfma16(f.D0, f.C1, acc[0][1]);
    acc[1][0] = mfma16(f.D1, f.C0, acc[1][0]);
    acc[1][1] = mfma16(f.D1, f.C1, acc[1][1]);
    acc[2][0] = mfma16(f.C0, f.D2, acc[2][0]);
    acc[2][1] = mfma16(f.C1, f.D2, acc[2][1]);
  };
  K1F F, G;
  wait_vm<4 * (K1_NS - 1)>();
  bar();
  TS(0, 1);
  rd(F, 0);
#pragma unroll 1
  for (int i = 0; i < 8; i += 2) {
    wait_tail<4, K1_NS - 2>(6 - i);
    bar();
    if (i + K1_NS < 8) issue(i + K1_NS);
    rd(G, i + 1);
    __builtin_amdgcn_sched_barrier(0);
    mm(F);
    if (i < 6) {
      wait_tail<4, K1_NS - 2>(5 - i);
      bar();
      if (i + 1 + K1_NS < 8) issue(i + 1 + K1_NS);
      rd(F, i + 2);
    }
    __builtin_amdgcn_sched_barrier(0);
    mm(G);
  }
  TS(0, 2);
  lds_char* st = smem + K1_NS * 32768;
  s1 = xsum16(xsum32(s1)); s2 = xsum16(xsum32(s2));
  cs[0] = xsum16(xsum32(cs[0])); cs[1] = xsum16(xsum32(cs[1])); cs[2] = xsum16(xsum32(cs[2]));
  if (g == 0) LDSF2(st + ((arow + tf * 16) * 2 + (hd >> 1)) * 8) = f32x2{s1, s2};
  else LDSF(st + 1024 + (((hd * 3 + g - 1) * 2 + rh) * 16 + c16) * 4) = g == 1 ? cs[0] : (g == 2 ? cs[1] : cs[2]);
  bar();
  lds_char* fin = smem + wv * 256;
  {
    f32x4 p = LDSF4(st + (rh * 32 + (l & 31)) * 16);
    float m = (p[0] + p[2]) * (1.f / DM);
    float var = fmaxf((p[1] + p[3]) * (1.f / DM) - m * m, 0.f);
    float r = __builtin_amdgcn_rsqf(var + 1e-5f);
    if (l < 32) LDSF2(fin + l * 8) = f32x2{r, -m * r};
  }
  const lds_char* cp = st + 1024 + hd * 3 * 128;
  f32x4 cq = LDSF4(cp + g * 16) + LDSF4(cp + 64 + g * 16);
  f32x4 ck = LDSF4(cp + 128 + g * 16) + LDSF4(cp + 192 + g * 16);
  float cv = LDSF(cp + 256 + c16 * 4) + LDSF(cp + 320 + c16 * 4);
  const int head = hg * 4 + hd, t32 = rb * 2 + rh;
  char* qo = (char*)qimg + (size_t)(head * 64 + t32) * 1024 + (g >> 1) * 512 + (g & 1) * 8;
  char* ko = (char*)kvimg + (size_t)(head * 64 + t32) * 2048;
  const float qs = 0.25f * 1.44269504088896340736f;
  u32x2 vp[2];
#pragma unroll
  for (int t = 0; t < 2; ++t) {
    const int tt = t ^ tf;
    f32x2 rm = LDSF2(fin + (tt * 16 + c16) * 8);
    f32x4 qv = (acc[0][t] * rm[0] + cq * rm[1]) * qs;
    f32x4 kv = acc[1][t] * rm[0] + ck * rm[1];
    *(u32x2*)(qo + (tt * 16 + c16) * 16) = u32x2{pack2(qv[0], qv[1]), pack2(qv[2], qv[3])};
    *(u32x2*)(ko + (g >> 1) * 512 + (g & 1) * 8 + (8 * q4 + 4 * tt + p4) * 16) = u32x2{pack2(kv[0], kv[1]), pack2(kv[2], kv[3])};
    f32x4 r01 = LDSF4(fin + (tt * 16 + 4 * g) * 8), r23 = LDSF4(fin + (tt * 16 + 4 * g) * 8 + 16);
    vp[t] = u32x2{pack2(acc[2][t][0] * r01[0] + cv * r01[1], acc[2][t][1] * r01[2] + cv * r01[3]),
                  pack2(acc[2][t][2] * r23[0] + cv * r23[1], acc[2][t][3] * r23[2] + cv * r23[3])};
  }
  u32x4 vv = tf ? u32x4{vp[1][0], vp[1][1], vp[0][0], vp[0][1]} : u32x4{vp[0][0], vp[0][1], vp[1][0], vp[1][1]};
  *(u32x4*)(ko + 1024 + l * 16) = vv;
  TS(0, 3);
}

DEV f32x16 pv(u32x4 v, f32x8 p, f32x16 o) {
  return __builtin_amdgcn_smfmac_f32_32x32x32_bf16(__builtin_bit_cast(bf16x8, v), __builtin_bit_cast(bf16x16, p), o, 0xdddd, 0, 0);
}
__global__ __launch_bounds__(512, 2) void k2_attn(const u16* __restrict__ qimg, const u16* __restrict__ kvimg,
                                                  u16* __restrict__ og) {
  __shared__ __attribute__((aligned(16))) char smem_[131072 + 4096 + 8192];
  lds_char* smem = (lds_char*)smem_;
  const int l = threadIdx.x & 63, wv = __builtin_amdgcn_readfirstlane(threadIdx.x >> 6);
  const int head = (blockIdx.x & 7) * 4 + ((blockIdx.x >> 3) & 3), qb = blockIdx.x >> 5;
  TS(1, 0);
  const char* gk = (const char*)kvimg + (size_t)head * 131072 + wv * 1024;
  const unsigned lbase = (unsigned)(uintptr_t)smem + wv * 1024;
  glds16((const char*)qimg + (size_t)(head * 64 + qb * 8 + wv) * 1024, l * 16, lbase + 135168);
#pragma unroll
  for (int s = 0; s < 16; ++s) glds16(gk + s * 8192, l * 16, lbase + s * 8192);
  *(__attribute__((address_space(3))) u32x2*)(smem + 131072 + threadIdx.x * 8) = u32x2{0x3f803f80u, 0x3f803f80u};
  const bool isv = (l & 16) == 0;
  const int va0 = isv ? 1024 + (l >> 5) * 256 + (l & 15) * 16 : 131072 + (l & 15) * 16;
  const int vstep = isv ? 4096 : 0;
  f32x16 cinit = {};
  f32x16 o;
  wait_vm<14>();
  bar();
  TS(1, 1);
  const u32x4 q = lds128(smem + 135168 + wv * 1024 + l * 16);
#pragma unroll 1
  for (int attempt = 0; attempt < 2; ++attempt) {
    o = f32x16{};
    int ka = l * 16, va = va0;
    f32x16 s0 = mfma32(lds128(smem + ka), q, cinit);
#pragma unroll 1
    for (int t = attempt * 64; t < attempt * 64 + 64; t += 2) {
      if (t == 6) { wait_vm<11>(); bar(); }
      if (t == 18) { wait_vm<0>(); bar(); }
      u32x4 k1 = lds128(smem + ka + 2048);
      u32x4 v00 = lds128(smem + va), v01 = lds128(smem + va + 512);
      u32x4 k2 = lds128(smem + ka + 4096);
      u32x4 v10 = lds128(smem + va + 2048), v11 = lds128(smem + va + 2560);
      f32x16 s1 = mfma32(k1, q, cinit);
      f32x8 p0, p1;
#pragma unroll
      for (int i = 0; i < 8; ++i) p0[i] = __builtin_amdgcn_exp2f(s0[i]);
      o = pv(v00, p0, o);
#pragma unroll
      for (int i = 0; i < 8; ++i) p1[i] = __builtin_amdgcn_exp2f(s0[8 + i]);
      o = pv(v01, p1, o);
      s0 = mfma32(k2, q, cinit);
#pragma unroll
      for (int i = 0; i < 8; ++i) p0[i] = __builtin_amdgcn_exp2f(s1[i]);
      o = pv(v10, p0, o);
#pragma unroll
      for (int i = 0; i < 8; ++i) p1[i] = __builtin_amdgcn_exp2f(s1[8 + i]);
      o = pv(v11, p1, o);
      ka += 4096; va += vstep;
    }
    if (attempt) break;
    float am = 0.f;
#pragma unroll
    for (int i = 0; i < 8; ++i) am = fmaxf(am, fabsf(o[i]));
    bool ok = o[8] >= 0x1p-60f && o[8] <= 0x1p100f && am <= 0x1p126f;
#pragma unroll
    for (int i = 0; i < 8; ++i) ok = ok && (o[i] == o[i]);
    if (__builtin_amdgcn_ballot_w64(!ok) == 0) break;
    float mx = -__builtin_inff();
#pragma unroll 1
    for (int t = 0; t < 64; ++t) {
      f32x16 s = mfma32(lds128(smem + t * 2048 + l * 16), q, f32x16{});
#pragma unroll
      for (int i = 0; i < 16; ++i) mx = fmaxf(mx, s[i]);
    }
    unsigned u = __builtin_bit_cast(unsigned, mx);
    auto r = __builtin_amdgcn_permlane32_swap(u, u, false, false);
    mx = fmaxf(u2f(r[0]), u2f(r[1]));
#pragma unroll
    for (int i = 0; i < 16; ++i) cinit[i] = -mx;
  }
  TS(1, 2);
  const float inv = 1.f / o[8];
  u32x4 ov = {pack2(o[0] * inv, o[1] * inv), pack2(o[2] * inv, o[3] * inv), pack2(o[4] * inv, o[5] * inv), pack2(o[6] * inv, o[7] * inv)};
  *(u32x4*)((char*)og + ((size_t)(head * 2 + (l >> 5)) * SEQ + qb * 256 + wv * 32 + (l & 31)) * 16) = ov;
  TS(1, 3);
}

struct KPF { u32x4 A0, B0, B1, A1, D0, D1; };
template <int KDIM, int NS, int MODE>
__global__ __launch_bounds__(512, 2) void k_proj(const u16* __restrict__ a, const u16* __restrict__ w,
                                                 const u16* __restrict__ res, u16* __restrict__ out) {
  constexpr int NT = KDIM / 64;
  __shared__ __attribute__((aligned(16))) char smem_[NS * 16384];
  lds_char* smem = (lds_char*)smem_;
  const int l = threadIdx.x & 63, wv = __builtin_amdgcn_readfirstlane(threadIdx.x >> 6);
  const int xcd = blockIdx.x & 7, j = blockIdx.x >> 3;
  TS(2 + 2 * MODE, 0);
  const int rb = xcd * 4 + (j & 3), cb = j >> 2;
  const int lr = wv * 8 + (l >> 3);
  const char* sa = (const char*)a + ((size_t)wv * SEQ + rb * 64) * 16;
  const char* sw = (const char*)w + cb * 128;
  const unsigned gb = lr * (DM * 2) + (((l & 7) ^ (2 * ((lr >> 1) & 3))) << 4);
  const unsigned lbase = (unsigned)(uintptr_t)smem + wv * 1024;
  int kt = (j * NT) >> 5;
  int slot = 0;
  auto issue = [&]() {
    unsigned d = lbase + slot * 16384;
    glds16(sa + (size_t)kt * (8 * SEQ * 16), l * 16, d);
    glds16(sw + (size_t)kt * (64 * DM * 2), gb, d + 8192);
    kt = kt + 1 == NT ? 0 : kt + 1;
    slot = slot + 1 == NS ? 0 : slot + 1;
  };
#pragma unroll
  for (int s = 0; s < NS; ++s) issue();
  const int g = l >> 4, c16 = l & 15;
  const int rt = wv & 3, ch = wv >> 2;
  const int tok = rb * 64 + rt * 16 + c16, col = cb * 64 + ch * 32 + (g & 1) * 16 + (g >> 1) * 8;
  const size_t off_rm = ((size_t)tok * DM + col) * 2, off_g8 = ((size_t)(col >> 3) * SEQ + tok) * 16;
  const int a0 = g * 1024 + (rt * 16 + c16) * 16;
  const int q4 = c16 >> 2, p4 = l & 3;
  const int brow = 16 * (g >> 1) + 4 * (g & 1) + q4;
  const int b0 = 8192 + brow * 128 + (((ch * 4 + (p4 >> 1)) ^ (2 * ((brow >> 1) & 3))) << 4) + (p4 & 1) * 8;
  const int b1 = b0 ^ 32;
  f32x4 acc0 = {}, acc1 = {};
  int cur = 0;
  auto rd = [&](KPF& f) {
    const lds_char* sb = smem + cur * 16384;
    cur = cur + 1 == NS ? 0 : cur + 1;
    f.A0 = lds128(sb + a0); f.B0 = lds_tr2(sb + b0, 1024); f.B1 = lds_tr2(sb + b1, 1024);
    f.A1 = lds128(sb + a0 + 4096); f.D0 = lds_tr2(sb + b0 + 4096, 1024); f.D1 = lds_tr2(sb + b1 + 4096, 1024);
  };
  auto mm = [&](const KPF& f) {
    acc0 = mfma16(f.B0, f.A0, acc0);
    acc1 = mfma16(f.B1, f.A0, acc1);
    acc0 = mfma16(f.D0, f.A1, acc0);
    acc1 = mfma16(f.D1, f.A1, acc1);
  };
  KPF F, G;
  u32x4 rv;
  wait_vm<2 * (NS - 1)>();
  bar();
  TS(2 + 2 * MODE, 1);
  rd(F);
#pragma unroll 1
  for (int i = 0; i < NT; i += 2) {
    wait_tail<2, NS - 2>(NT - 2 - i);
    bar();
    if (i + NS < NT) issue();
    rd(G);
    __builtin_amdgcn_sched_barrier(0);
    mm(F);
    if (i < NT - 2) {
      wait_tail<2, NS - 2>(NT - 3 - i);
      bar();
      if (i + 1 + NS < NT) issue();
      rd(F);
    }
    __builtin_amdgcn_sched_barrier(0);
    mm(G);
  }
  TS(2 + 2 * MODE, 2);
  rv = *(const u32x4*)((const char*)res + (MODE == 0 ? off_rm : off_g8));
  float v[8];
#pragma unroll
  for (int i = 0; i < 4; ++i) {
    auto r = __builtin_amdgcn_permlane16_swap(f2u(acc0[i]), f2u(acc1[i]), false, false);
    v[i] = u2f(r[0]);
    v[4 + i] = u2f(r[1]);
  }
  u32x4 ov;
#pragma unroll
  for (int i = 0; i < 4; ++i) ov[i] = pack2(v[2 * i] + bflo(rv[i]), v[2 * i + 1] + bfhi(rv[i]));
  *(u32x4*)((char*)out + (MODE == 0 ? off_g8 : off_rm)) = ov;
  TS(2 + 2 * MODE, 3);
}

#define K4_NS 6
struct K4F { u32x4 A0, G0, A1, U0, C0, G1, C1, U1; };
__global__ __launch_bounds__(512, 2) void k4_gateup(const u16* __restrict__ h, const u16* __restrict__ wg,
                                                    const u16* __restrict__ wu, u16* __restrict__ act) {
  __shared__ __attribute__((aligned(16))) char smem_[K4_NS * 24576 + 4096];
  lds_char* smem = (lds_char*)smem_;
  const int l = threadIdx.x & 63, wv = __builtin_amdgcn_readfirstlane(threadIdx.x >> 6);
  const int xcd = blockIdx.x & 7, j = blockIdx.x >> 3;
  TS(3, 0);
  const int rb = (xcd >> 1) * 4 + (j & 3), cb = (xcd & 1) * 7 + (j >> 2);
  const char* sa = (const char*)h + ((size_t)(wv >> 1) * SEQ + rb * 128 + (wv & 1) * 64) * 16;
  const int lr = wv * 4 + (l >> 4);
  const unsigned gb = lr * (FF * 2) + (((l & 15) ^ (4 * (lr & 3))) << 4);
  const char* sg = (const char*)wg + cb * 256;
  const char* su = (const char*)wu + cb * 256;
  const unsigned lbase = (unsigned)(uintptr_t)smem + wv * 1024;
  int kt = (j * 16) / 28;
  int slot = 0;
  auto issue = [&]() {
    unsigned d = lbase + slot * 24576;
    glds16(sa + (size_t)kt * (4 * SEQ * 16), l * 16, d);
    glds16(sg + (size_t)kt * (32 * FF * 2), gb, d + 8192);
    glds16(su + (size_t)kt * (32 * FF * 2), gb, d + 16384);
    kt = (kt + 1) & 15;
    slot = slot + 1 == K4_NS ? 0 : slot + 1;
  };
#pragma unroll
  for (int s = 0; s < K4_NS; ++s) issue();
  const int rh = wv & 1, cg = wv >> 1;
  const int ksf = rh ^ (cg >> 1), tf = cg & 1;
  const int hb = l >> 5, r32 = l & 31;
  const int a00 = (ksf * 2 + hb) * 2048 + (rh * 64 + tf * 32 + r32) * 16;
  const int a01 = a00 ^ 512, a10 = a00 ^ 4096, a11 = a00 ^ 4096 ^ 512;
  const int q4 = (l & 15) >> 2, p4 = l & 3;
  const int b0 = 8192 + (ksf * 16 + hb * 8 + q4) * 256 + (((cg * 4 + ((l >> 4) & 1) * 2 + (p4 >> 1)) ^ (4 * q4)) << 4) + (p4 & 1) * 8;
  const int b1 = b0 ^ 4096;
  f32x16 ag0 = {}, ag1 = {}, au0 = {}, au1 = {};
  float s1 = 0.f, s2 = 0.f, csg = 0.f, csu = 0.f;
  int cur = 0;
  auto rd = [&](K4F& f) {
    const lds_char* sb = smem + cur * 24576;
    cur = cur + 1 == K4_NS ? 0 : cur + 1;
    f.A0 = lds128(sb + a00); f.G0 = lds_tr2(sb + b0, 1024); f.A1 = lds128(sb + a01); f.U0 = lds_tr2(sb + b0 + 8192, 1024);
    f.C0 = lds128(sb + a10); f.G1 = lds_tr2(sb + b1, 1024); f.C1 = lds128(sb + a11); f.U1 = lds_tr2(sb + b1 + 8192, 1024);
  };
  auto mm = [&](const K4F& f) {
    ag0 = mfma32(f.G0, f.A0, ag0);
    s1 = sum8(f.A0, s1);
    ag1 = mfma32(f.G0, f.A1, ag1);
    s2 = sq8(f.A0, s2);
    au0 = mfma32(f.U0, f.A0, au0);
    csg = sum8(f.G0, csg);
    au1 = mfma32(f.U0, f.A1, au1);
    csu = sum8(f.U0, csu);
    ag0 = mfma32(f.G1, f.C0, ag0);
    ag1 = mfma32(f.G1, f.C1, ag1);
    au0 = mfma32(f.U1, f.C0, au0);
    au1 = mfma32(f.U1, f.C1, au1);
  };
  K4F F, G;
  wait_vm<3 * (K4_NS - 1)>();
  bar();
  TS(3, 1);
  rd(F);
#pragma unroll 1
  for (int i = 0; i < 16; i += 2) {
    wait_tail<3, K4_NS - 2>(14 - i);
    bar();
    if (i + K4_NS < 16) issue();
    rd(G);
    __builtin_amdgcn_sched_barrier(0);
    mm(F);
    if (i < 14) {
      wait_tail<3, K4_NS - 2>(13 - i);
      bar();
      if (i + 1 + K4_NS < 16) issue();
      rd(F);
    }
    __builtin_amdgcn_sched_barrier(0);
    mm(G);
  }
  TS(3, 2);
  lds_char* st = smem + K4_NS * 24576;
  s1 = xsum32(s1); s2 = xsum32(s2); csg = xsum32(csg); csu = xsum32(csu);
  if (hb == 0) LDSF2(st + ((rh * 64 + tf * 32 + r32) * 2 + (cg >> 1)) * 8) = f32x2{s1, s2};
  LDSF(st + 2048 + (((cg * 2 + hb) * 2 + rh) * 32 + r32) * 4) = hb ? csu : csg;
  bar();
  const lds_char* cp = st + 2048 + cg * 512 + hb * 16;
  char* ao = (char*)act + ((size_t)(cb * 16 + cg * 4 + hb) * SEQ + rb * 128 + rh * 64 + r32) * 16;
#pragma unroll
  for (int t = 0; t < 2; ++t) {
    const int tt = t ^ tf;
    f32x4 p = LDSF4(st + (rh * 64 + tt * 32 + r32) * 16);
    float m = (p[0] + p[2]) * (1.f / DM);
    float var = fmaxf((p[1] + p[3]) * (1.f / DM) - m * m, 0.f);
    float r = __builtin_amdgcn_rsqf(var + 1e-5f);
    float mr = -m * r;
    unsigned pk[8];
#pragma unroll
    for (int c = 0; c < 4; ++c) {
      f32x4 cgv = LDSF4(cp + c * 32) + LDSF4(cp + 128 + c * 32);
      f32x4 cuv = LDSF4(cp + 256 + c * 32) + LDSF4(cp + 384 + c * 32);
      float y[4];
#pragma unroll
      for (int e = 0; e < 4; ++e) {
        float gv = (t ? ag1[c * 4 + e] : ag0[c * 4 + e]) * r + cgv[e] * mr;
        float uv = (t ? au1[c * 4 + e] : au0[c * 4 + e]) * r + cuv[e] * mr;
        float sg_ = __builtin_amdgcn_rcpf(1.f + __builtin_amdgcn_exp2f(gv * -1.44269504088896340736f));
        y[e] = gv * sg_ * uv;
      }
      pk[c * 2] = pack2(y[0], y[1]);
      pk[c * 2 + 1] = pack2(y[2], y[3]);
    }
    *(u32x4*)(ao + tt * 512) = u32x4{pk[0], pk[1], pk[2], pk[3]};
    *(u32x4*)(ao + tt * 512 + 2 * SEQ * 16) = u32x4{pk[4], pk[5], pk[6], pk[7]};
  }
  TS(3, 3);
}

extern "C" void kernel_launch(void* const* d_in, const int* in_sizes, int n_in, void* d_out, int out_size,
                              void* d_ws, size_t ws_size, hipStream_t stream) {
  const u16* x = (const u16*)d_in[0];
  const u16* wqkv = (const u16*)d_in[1];
  const u16* wo = (const u16*)d_in[2];
  const u16* wg = (const u16*)d_in[4];
  const u16* wu = (const u16*)d_in[5];
  const u16* wd = (const u16*)d_in[6];
  char* ws = (char*)d_ws;
  u16* qimg = (u16*)ws;
  u16* kvimg = (u16*)(ws + (2u << 20));
  u16* og = (u16*)(ws + (6u << 20));
  u16* h = (u16*)(ws + (8u << 20));
  u16* act = (u16*)(ws + (10u << 20));
  k1_qkv<<<256, 512, 0, stream>>>(x, wqkv, qimg, kvimg);
  k2_attn<<<256, 512, 0, stream>>>(qimg, kvimg, og);
  k_proj<512, 8, 0><<<256, 512, 0, stream>>>(og, wo, x, h);
  k4_gateup<<<224, 512, 0, stream>>>(h, wg, wu, act);
  k_proj<1792, 9, 1><<<256, 512, 0, stream>>>(act, wd, h, (u16*)d_out);
}
```

```cpp
#include <hip/hip_runtime.h>
#include <hip/hip_bf16.h>
#include <stdint.h>

typedef __attribute__((ext_vector_type(2))) __bf16 bf16x2;
typedef __attribute__((ext_vector_type(8))) __bf16 bf16x8;
typedef __attribute__((ext_vector_type(16))) __bf16 bf16x16;
typedef __attribute__((ext_vector_type(4))) short s16x4;
typedef __attribute__((ext_vector_type(2))) float f32x2;
typedef __attribute__((ext_vector_type(4))) float f32x4;
typedef __attribute__((ext_vector_type(8))) float f32x8;
typedef __attribute__((ext_vector_type(16))) float f32x16;
typedef __attribute__((ext_vector_type(2))) unsigned u32x2;
typedef __attribute__((ext_vector_type(4))) unsigned u32x4;
typedef __attribute__((address_space(3))) char lds_char;
typedef unsigned short u16;

#define DEV __device__ __forceinline__
#define TSB
#define TS(i)
#define TSE(k)
#define SEQ 2048
#define DM 512
#define FF 1792

DEV void glds16(const void* sbase, unsigned voff, unsigned ldsaddr) {
  asm volatile("s_mov_b32 m0, %2\n\ts_nop 0\n\tglobal_load_lds_dwordx4 %0, %1" ::"v"(voff), "s"(sbase), "s"(ldsaddr) : "memory");
}
template <int N> DEV void wait_vm() { asm volatile("s_waitcnt vmcnt(%0)" ::"n"(N) : "memory"); }
template <int P, int D> DEV void wait_tail(int rem) {
  asm volatile(
      "s_waitcnt vmcnt(%1)\n\ts_cmp_ge_i32 %0, %2\n\ts_cbranch_scc1 1f\n\t"
      "s_waitcnt vmcnt(%3)\n\ts_cmp_ge_i32 %0, 3\n\ts_cbranch_scc1 1f\n\t"
      "s_waitcnt vmcnt(%4)\n\ts_cmp_ge_i32 %0, 2\n\ts_cbranch_scc1 1f\n\t"
      "s_waitcnt vmcnt(%5)\n\ts_cmp_ge_i32 %0, 1\n\ts_cbranch_scc1 1f\n\t"
      "s_waitcnt vmcnt(0)\n1:" ::"s"(rem), "n"(P * D), "n"(D), "n"(P * (D < 3 ? D : 3)), "n"(P * (D < 2 ? D : 2)), "n"(P)
      : "memory", "scc");
}
DEV void bar() {
  asm volatile("s_waitcnt lgkmcnt(0)" ::: "memory");
  __builtin_amdgcn_s_barrier();
  asm volatile("" ::: "memory");
}
DEV u32x4 lds128(const lds_char* p) { return *(const __attribute__((address_space(3))) u32x4*)p; }
DEV u32x2 lds_tr(const lds_char* p) {
  s16x4 v = __builtin_amdgcn_ds_read_tr16_b64_v4i16((__attribute__((address_space(3))) s16x4*)p);
  return __builtin_bit_cast(u32x2, v);
}
DEV u32x4 lds_tr2(const lds_char* p, int second) {
  u32x2 a = lds_tr(p), b = lds_tr(p + second);
  return u32x4{a[0], a[1], b[0], b[1]};
}
DEV f32x4 mfma16(u32x4 a, u32x4 b, f32x4 c) {
  return __builtin_amdgcn_mfma_f32_16x16x32_bf16(__builtin_bit_cast(bf16x8, a), __builtin_bit_cast(bf16x8, b), c, 0, 0, 0);
}
DEV f32x16 mfma32(u32x4 a, u32x4 b, f32x16 c) {
  return __builtin_amdgcn_mfma_f32_32x32x16_bf16(__builtin_bit_cast(bf16x8, a), __builtin_bit_cast(bf16x8, b), c, 0, 0, 0);
}
DEV float dot2(unsigned a, unsigned b, float c) {
  return __builtin_amdgcn_fdot2_f32_bf16(__builtin_bit_cast(bf16x2, a), __builtin_bit_cast(bf16x2, b), c, false);
}
DEV float sum8(u32x4 v, float c) {
  c = dot2(v[0], 0x3f803f80u, c); c = dot2(v[1], 0x3f803f80u, c);
  c = dot2(v[2], 0x3f803f80u, c); c = dot2(v[3], 0x3f803f80u, c);
  return c;
}
DEV float sq8(u32x4 v, float c) {
  c = dot2(v[0], v[0], c); c = dot2(v[1], v[1], c); c = dot2(v[2], v[2], c); c = dot2(v[3], v[3], c);
  return c;
}
DEV unsigned pack2(float lo, float hi) {
  bf16x2 v = {(__bf16)lo, (__bf16)hi};
  return __builtin_bit_cast(unsigned, v);
}
DEV float u2f(unsigned u) { return __builtin_bit_cast(float, u); }
DEV unsigned f2u(float f) { return __builtin_bit_cast(unsigned, f); }
DEV float bflo(unsigned u) { return __builtin_bit_cast(float, u << 16); }
DEV float bfhi(unsigned u) { return __builtin_bit_cast(float, u & 0xffff0000u); }
DEV float xsum32(float v) {
  unsigned u = __builtin_bit_cast(unsigned, v);
  auto r = __builtin_amdgcn_permlane32_swap(u, u, false, false);
  return u2f(r[0]) + u2f(r[1]);
}
DEV float xsum16(float v) {
  unsigned u = __builtin_bit_cast(unsigned, v);
  auto r = __builtin_amdgcn_permlane16_swap(u, u, false, false);
  return u2f(r[0]) + u2f(r[1]);
}
#define LDSF(p) (*(__attribute__((address_space(3))) float*)(p))
#define LDSF2(p) (*(__attribute__((address_space(3))) f32x2*)(p))
#define LDSF4(p) (*(__attribute__((address_space(3))) f32x4*)(p))

#define K1_NS 4
struct K1F { u32x4 A0, A1, B0, B1, B2, C0, C1, D0, D1, D2; };
__global__ __launch_bounds__(512, 2) void k1_qkv(const u16* __restrict__ x, const u16* __restrict__ w,
                                                 u16* __restrict__ qimg, u16* __restrict__ kvimg) {
  __shared__ __attribute__((aligned(16))) char smem_[K1_NS * 32768 + 4096];
  lds_char* smem = (lds_char*)smem_;
  const int l = threadIdx.x & 63, wv = __builtin_amdgcn_readfirstlane(threadIdx.x >> 6);
  TSB;
  const int hg = blockIdx.x & 7, rb = blockIdx.x >> 3;
  const int rh = wv & 1, hd = wv >> 1;
  const int lr = wv * 8 + (l >> 3);
  const unsigned ga = lr * (DM * 2) + (((l & 7) ^ ((lr >> 1) & 7)) << 4);
  const unsigned gb = lr * (1536 * 2) + (((l & 7) ^ (2 * (((lr >> 1) & 1) | (((lr >> 3) & 1) << 1)))) << 4);
  const char* xa = (const char*)x + (size_t)rb * (64 * DM * 2);
  const char* wb = (const char*)w + hg * 128;
  const unsigned lbase = (unsigned)(uintptr_t)smem + wv * 1024;
  const int rot = rb & 7;
  auto issue = [&](int st) {
    int kt = (st + rot) & 7;
    unsigned d = lbase + (st % K1_NS) * 32768;
    glds16(xa + kt * 128, ga, d);
    const char* b = wb + (size_t)kt * (64 * 1536 * 2);
    glds16(b, gb, d + 8192);
    glds16(b + 1024, gb, d + 16384);
    glds16(b + 2048, gb, d + 24576);
  };
#pragma unroll
  for (int s = 0; s < K1_NS; ++s) issue(s);

  const int g = l >> 4, c16 = l & 15;
  const int ksf = rh ^ (hd >> 1), tf = hd & 1;
  const int arow = rh * 32 + c16;
  const int a00 = (arow + tf * 16) * 128 + (((ksf * 4 + g) ^ (c16 >> 1)) << 4);
  const int a01 = a00 ^ 2048, a10 = a00 ^ 64, a11 = a00 ^ 2048 ^ 64;
  const int q4 = c16 >> 2, p4 = l & 3;
  const int b0 = 8192 + (ksf * 32 + 8 * g + q4) * 128 + (((hd * 2 + (p4 >> 1)) ^ (2 * ((q4 >> 1) | ((g & 1) << 1)))) << 4) + (p4 & 1) * 8;
  const int b1 = b0 ^ 4096;
  f32x4 acc[3][2] = {};
  float s1 = 0.f, s2 = 0.f, cs[3] = {0.f, 0.f, 0.f};
  auto rd = [&](K1F& f, int st) {
    const lds_char* sb = smem + (st % K1_NS) * 32768;
    f.A0 = lds128(sb + a00); f.B0 = lds_tr2(sb + b0, 512); f.A1 = lds128(sb + a01);
    f.B1 = lds_tr2(sb + b0 + 8192, 512); f.B2 = lds_tr2(sb + b0 + 16384, 512);
    f.C0 = lds128(sb + a10); f.D0 = lds_tr2(sb + b1, 512); f.C1 = lds128(sb + a11);
    f.D1 = lds_tr2(sb + b1 + 8192, 512); f.D2 = lds_tr2(sb + b1 + 16384, 512);
  };
  auto mm = [&](const K1F& f) {
    acc[0][0] = mfma16(f.B0, f.A0, acc[0][0]);
    acc[0][1] = mfma16(f.B0, f.A1, acc[0][1]);
    acc[1][0] = mfma16(f.B1, f.A0, acc[1][0]);
    acc[1][1] = mfma16(f.B1, f.A1, acc[1][1]);
    acc[2][0] = mfma16(f.A0, f.B2, acc[2][0]);
    acc[2][1] = mfma16(f.A1, f.B2, acc[2][1]);
    s1 = sum8(f.A0, s1); s2 = sq8(f.A0, s2);
    cs[0] = sum8(f.B0, cs[0]); cs[1] = sum8(f.B1, cs[1]); cs[2] = sum8(f.B2, cs[2]);
    acc[0][0] = mfma16(f.D0, f.C0, acc[0][0]);
    acc[0][1] = mfma16(f.D0, f.C1, acc[0][1]);
    acc[1][0] = mfma16(f.D1, f.C0, acc[1][0]);
    acc[1][1] = mfma16(f.D1, f.C1, acc[1][1]);
    acc[2][0] = mfma16(f.C0, f.D2, acc[2][0]);
    acc[2][1] = mfma16(f.C1, f.D2, acc[2][1]);
  };
  K1F F, G;
  wait_vm<4 * (K1_NS - 1)>();
  bar();
  TS(1);
  rd(F, 0);
#pragma unroll 1
  for (int i = 0; i < 8; i += 2) {
    wait_tail<4, K1_NS - 2>(6 - i);
    bar();
    if (i + K1_NS < 8) issue(i + K1_NS);
    rd(G, i + 1);
    __builtin_amdgcn_sched_barrier(0);
    mm(F);
    if (i < 6) {
      wait_tail<4, K1_NS - 2>(5 - i);
      bar();
      if (i + 1 + K1_NS < 8) issue(i + 1 + K1_NS);
      rd(F, i + 2);
    }
    __builtin_amdgcn_sched_barrier(0);
    mm(G);
  }
  TS(2);
  lds_char* st = smem + K1_NS * 32768;
  s1 = xsum16(xsum32(s1)); s2 = xsum16(xsum32(s2));
  cs[0] = xsum16(xsum32(cs[0])); cs[1] = xsum16(xsum32(cs[1])); cs[2] = xsum16(xsum32(cs[2]));
  if (g == 0) LDSF2(st + ((arow + tf * 16) * 2 + (hd >> 1)) * 8) = f32x2{s1, s2};
  else LDSF(st + 1024 + (((hd * 3 + g - 1) * 2 + rh) * 16 + c16) * 4) = g == 1 ? cs[0] : (g == 2 ? cs[1] : cs[2]);
  bar();
  lds_char* fin = smem + wv * 256;
  {
    f32x4 p = LDSF4(st + (rh * 32 + (l & 31)) * 16);
    float m = (p[0] + p[2]) * (1.f / DM);
    float var = fmaxf((p[1] + p[3]) * (1.f / DM) - m * m, 0.f);
    float r = __builtin_amdgcn_rsqf(var + 1e-5f);
    if (l < 32) LDSF2(fin + l * 8) = f32x2{r, -m * r};
  }
  const lds_char* cp = st + 1024 + hd * 3 * 128;
  f32x4 cq = LDSF4(cp + g * 16) + LDSF4(cp + 64 + g * 16);
  f32x4 ck = LDSF4(cp + 128 + g * 16) + LDSF4(cp + 192 + g * 16);
  float cv = LDSF(cp + 256 + c16 * 4) + LDSF(cp + 320 + c16 * 4);
  const int head = hg * 4 + hd, t32 = rb * 2 + rh;
  char* qo = (char*)qimg + (size_t)(head * 64 + t32) * 1024 + (g >> 1) * 512 + (g & 1) * 8;
  char* ko = (char*)kvimg + (size_t)(head * 64 + t32) * 2048;
  const float qs = 0.25f * 1.44269504088896340736f;
  u32x2 vp[2];
#pragma unroll
  for (int t = 0; t < 2; ++t) {
    const int tt = t ^ tf;
    f32x2 rm = LDSF2(fin + (tt * 16 + c16) * 8);
    f32x4 qv = (acc[0][t] * rm[0] + cq * rm[1]) * qs;
    f32x4 kv = acc[1][t] * rm[0] + ck * rm[1];
    *(u32x2*)(qo + (tt * 16 + c16) * 16) = u32x2{pack2(qv[0], qv[1]), pack2(qv[2], qv[3])};
    *(u32x2*)(ko + (g >> 1) * 512 + (g & 1) * 8 + (8 * q4 + 4 * tt + p4) * 16) = u32x2{pack2(kv[0], kv[1]), pack2(kv[2], kv[3])};
    f32x4 r01 = LDSF4(fin + (tt * 16 + 4 * g) * 8), r23 = LDSF4(fin + (tt * 16 + 4 * g) * 8 + 16);
    vp[t] = u32x2{pack2(acc[2][t][0] * r01[0] + cv * r01[1], acc[2][t][1] * r01[2] + cv * r01[3]),
                  pack2(acc[2][t][2] * r23[0] + cv * r23[1], acc[2][t][3] * r23[2] + cv * r23[3])};
  }
  u32x4 vv = tf ? u32x4{vp[1][0], vp[1][1], vp[0][0], vp[0][1]} : u32x4{vp[0][0], vp[0][1], vp[1][0], vp[1][1]};
  *(u32x4*)(ko + 1024 + l * 16) = vv;
  TSE(0);
}

DEV f32x16 pv(u32x4 v, f32x8 p, f32x16 o) {
  return __builtin_amdgcn_smfmac_f32_32x32x32_bf16(__builtin_bit_cast(bf16x8, v), __builtin_bit_cast(bf16x16, p), o, 0xdddd, 0, 0);
}
__global__ __launch_bounds__(512, 2) void k2_attn(const u16* __restrict__ qimg, const u16* __restrict__ kvimg,
                                                  u16* __restrict__ og) {
  __shared__ __attribute__((aligned(16))) char smem_[131072 + 4096 + 8192];
  lds_char* smem = (lds_char*)smem_;
  const int l = threadIdx.x & 63, wv = __builtin_amdgcn_readfirstlane(threadIdx.x >> 6);
  const int head = (blockIdx.x & 7) * 4 + ((blockIdx.x >> 3) & 3), qb = blockIdx.x >> 5;
  TSB;
  const char* gk = (const char*)kvimg + (size_t)head * 131072 + wv * 1024;
  const unsigned lbase = (unsigned)(uintptr_t)smem + wv * 1024;
  glds16((const char*)qimg + (size_t)(head * 64 + qb * 8 + wv) * 1024, l * 16, lbase + 135168);
#pragma unroll
  for (int s = 0; s < 16; ++s) {
    glds16(gk + s * 8192, l * 16, lbase + s * 8192);
    if (s == 1 || s == 4) __builtin_amdgcn_s_barrier();
  }
  *(__attribute__((address_space(3))) u32x2*)(smem + 131072 + threadIdx.x * 8) = u32x2{0x3f803f80u, 0x3f803f80u};
  const bool isv = (l & 16) == 0;
  const int va0 = isv ? 1024 + (l >> 5) * 256 + (l & 15) * 16 : 131072 + (l & 15) * 16;
  const int vstep = isv ? 4096 : 0;
  f32x16 cinit = {};
  f32x16 o;
  wait_vm<14>();
  bar();
  TS(1);
  const u32x4 q = lds128(smem + 135168 + wv * 1024 + l * 16);
#pragma unroll 1
  for (int attempt = 0; attempt < 2; ++attempt) {
    o = f32x16{};
    int ka = (unsigned)(uintptr_t)smem + l * 16, va = (unsigned)(uintptr_t)smem + va0;
    int cnt = attempt ? 32 : 3, seg = attempt ? 2 : 0;
#define EX(r) "v_exp_f32 v" #r ", v" #r "\n\t"
#define PV(V, P) "s_waitcnt lgkmcnt(2)\n\tv_smfmac_f32_32x32x32_bf16 v[16:31], " V ", " P ", %[idx]\n\t"
#define QK(S) "s_waitcnt lgkmcnt(2)\n\tv_mfma_f32_32x32x16_bf16 " S ", v[84:87], v[80:83], v[32:47]\n\t"
    asm volatile(
        "ds_read_b128 v[84:87], %[ka]\n\t"
        "v_mov_b32 v72, 0\n\tv_mov_b32 v73, 0\n\tv_mov_b32 v74, 0\n\tv_mov_b32 v75, 0\n\t"
        "v_mov_b32 v76, 0\n\tv_mov_b32 v77, 0\n\tv_mov_b32 v78, 0\n\tv_mov_b32 v79, 0\n\t"
        "v_mov_b32 v92, 0\n\tv_mov_b32 v93, 0\n\tv_mov_b32 v94, 0\n\tv_mov_b32 v95, 0\n\t"
        "s_waitcnt lgkmcnt(0)\n\t"
        "v_mfma_f32_32x32x16_bf16 v[48:63], v[84:87], v[80:83], v[32:47]\n\t"
        "ds_read_b128 v[84:87], %[ka] offset:2048\n\t"
        "ds_read_b128 v[88:91], %[va]\n\t"
        "s_nop 10\n"
        "1:\n\t"
        EX(48) EX(49) PV("v[92:95]", "v[72:79]")
        "ds_read_b128 v[92:95], %[va] offset:512\n\t"
        EX(50) EX(51) EX(52) EX(53) EX(54) QK("v[64:79]")
        "ds_read_b128 v[84:87], %[ka] offset:4096\n\t"
        EX(55) EX(56) EX(57) EX(58) EX(59) EX(60) PV("v[88:91]", "v[48:55]")
        "ds_read_b128 v[88:91], %[va] offset:2048\n\t"
        EX(61) EX(62) EX(63)
        EX(64) EX(65) PV("v[92:95]", "v[56:63]")
        "ds_read_b128 v[92:95], %[va] offset:2560\n\t"
        "v_add_u32 %[va], %[va], %[vs]\n\t"
        EX(66) EX(67) EX(68) EX(69) EX(70) QK("v[48:63]")
        "ds_read_b128 v[84:87], %[ka] offset:6144\n\t"
        "v_add_u32 %[ka], 0x1000, %[ka]\n\t"
        EX(71) EX(72) EX(73) EX(74) EX(75) EX(76) PV("v[88:91]", "v[64:71]")
        "ds_read_b128 v[88:91], %[va]\n\t"
        "s_sub_u32 %[cnt], %[cnt], 1\n\t"
        "s_cmp_lg_u32 %[cnt], 0\n\t"
        EX(77) EX(78) EX(79)
        "s_cbranch_scc1 1b\n\t"
        "s_cmp_eq_u32 %[seg], 2\n\t"
        "s_cbranch_scc1 2f\n\t"
        "s_waitcnt vmcnt(11)\n\t"
        "s_mov_b32 %[cnt], 6\n\t"
        "s_add_u32 %[seg], %[seg], 1\n\t"
        "s_cmp_eq_u32 %[seg], 1\n\t"
        "s_cbranch_scc1 3f\n\t"
        "s_waitcnt vmcnt(0)\n\t"
        "s_mov_b32 %[cnt], 23\n"
        "3:\n\t"
        "s_barrier\n\t"
        "s_branch 1b\n"
        "2:\n\t"
        "s_waitcnt lgkmcnt(0)\n\t"
        "v_smfmac_f32_32x32x32_bf16 v[16:31], v[92:95], v[72:79], %[idx]\n\t"
        "s_nop 15\n\t"
        : "+{v[16:31]}"(o), [ka] "+v"(ka), [va] "+v"(va), [cnt] "+s"(cnt), [seg] "+s"(seg)
        : "{v[32:47]}"(cinit), "{v[80:83]}"(q), [vs] "v"(vstep), [idx] "v"(0xdddd)
        : "memory", "scc", "v48", "v49", "v50", "v51", "v52", "v53", "v54", "v55", "v56", "v57", "v58", "v59", "v60", "v61",
          "v62", "v63", "v64", "v65", "v66", "v67", "v68", "v69", "v70", "v71", "v72", "v73", "v74", "v75", "v76", "v77",
          "v78", "v79", "v84", "v85", "v86", "v87", "v88", "v89", "v90", "v91", "v92", "v93", "v94", "v95");
    if (attempt) break;
    float am = 0.f;
#pragma unroll
    for (int i = 0; i < 8; ++i) am = fmaxf(am, fabsf(o[i]));
    bool ok = o[8] >= 0x1p-60f && o[8] <= 0x1p100f && am <= 0x1p126f;
#pragma unroll
    for (int i = 0; i < 8; ++i) ok = ok && (o[i] == o[i]);
    if (__builtin_amdgcn_ballot_w64(!ok) == 0) break;
    float mx = -__builtin_inff();
#pragma unroll 1
    for (int t = 0; t < 64; ++t) {
      f32x16 s = mfma32(lds128(smem + t * 2048 + l * 16), q, f32x16{});
#pragma unroll
      for (int i = 0; i < 16; ++i) mx = fmaxf(mx, s[i]);
    }
    unsigned u = __builtin_bit_cast(unsigned, mx);
    auto r = __builtin_amdgcn_permlane32_swap(u, u, false, false);
    mx = fmaxf(u2f(r[0]), u2f(r[1]));
#pragma unroll
    for (int i = 0; i < 16; ++i) cinit[i] = -mx;
  }
  TS(2);
  const float inv = 1.f / o[8];
  u32x4 ov = {pack2(o[0] * inv, o[1] * inv), pack2(o[2] * inv, o[3] * inv), pack2(o[4] * inv, o[5] * inv), pack2(o[6] * inv, o[7] * inv)};
  *(u32x4*)((char*)og + ((size_t)(head * 2 + (l >> 5)) * SEQ + qb * 256 + wv * 32 + (l & 31)) * 16) = ov;
  TSE(1);
}

struct KPF { u32x4 A0, B0, B1, A1, D0, D1; };
template <int KDIM, int NS, int MODE>
__global__ __launch_bounds__(512, 2) void k_proj(const u16* __restrict__ a, const u16* __restrict__ w,
                                                 const u16* __restrict__ res, u16* __restrict__ out) {
  constexpr int NT = KDIM / 64;
  __shared__ __attribute__((aligned(16))) char smem_[NS * 16384];
  lds_char* smem = (lds_char*)smem_;
  const int l = threadIdx.x & 63, wv = __builtin_amdgcn_readfirstlane(threadIdx.x >> 6);
  const int xcd = blockIdx.x & 7, j = blockIdx.x >> 3;
  TSB;
  const int rb = xcd * 4 + (j & 3), cb = j >> 2;
  const int lr = wv * 8 + (l >> 3);
  const char* sa = (const char*)a + ((size_t)wv * SEQ + rb * 64) * 16;
  const char* sw = (const char*)w + cb * 128;
  const unsigned gb = lr * (DM * 2) + (((l & 7) ^ (2 * ((lr >> 1) & 3))) << 4);
  const unsigned lbase = (unsigned)(uintptr_t)smem + wv * 1024;
  int kt = (j * NT) >> 5;
  int slot = 0;
  auto issue = [&]() {
    unsigned d = lbase + slot * 16384;
    glds16(sa + (size_t)kt * (8 * SEQ * 16), l * 16, d);
    glds16(sw + (size_t)kt * (64 * DM * 2), gb, d + 8192);
    kt = kt + 1 == NT ? 0 : kt + 1;
    slot = slot + 1 == NS ? 0 : slot + 1;
  };
#pragma unroll
  for (int s = 0; s < NS; ++s) issue();
  const int g = l >> 4, c16 = l & 15;
  const int rt = wv & 3, ch = wv >> 2;
  const int tok = rb * 64 + rt * 16 + c16, col = cb * 64 + ch * 32 + (g & 1) * 16 + (g >> 1) * 8;
  const size_t off_rm = ((size_t)tok * DM + col) * 2, off_g8 = ((size_t)(col >> 3) * SEQ + tok) * 16;
  const int a0 = g * 1024 + (rt * 16 + c16) * 16;
  const int q4 = c16 >> 2, p4 = l & 3;
  const int brow = 16 * (g >> 1) + 4 * (g & 1) + q4;
  const int b0 = 8192 + brow * 128 + (((ch * 4 + (p4 >> 1)) ^ (2 * ((brow >> 1) & 3))) << 4) + (p4 & 1) * 8;
  const int b1 = b0 ^ 32;
  f32x4 acc0 = {}, acc1 = {};
  int cur = 0;
  auto rd = [&](KPF& f) {
    const lds_char* sb = smem + cur * 16384;
    cur = cur + 1 == NS ? 0 : cur + 1;
    f.A0 = lds128(sb + a0); f.B0 = lds_tr2(sb + b0, 1024); f.B1 = lds_tr2(sb + b1, 1024);
    f.A1 = lds128(sb + a0 + 4096); f.D0 = lds_tr2(sb + b0 + 4096, 1024); f.D1 = lds_tr2(sb + b1 + 4096, 1024);
  };
  auto mm = [&](const KPF& f) {
    acc0 = mfma16(f.B0, f.A0, acc0);
    acc1 = mfma16(f.B1, f.A0, acc1);
    acc0 = mfma16(f.D0, f.A1, acc0);
    acc1 = mfma16(f.D1, f.A1, acc1);
  };
  KPF F, G;
  u32x4 rv;
  wait_vm<2 * (NS - 1)>();
  bar();
  TS(1);
  rd(F);
#pragma unroll 1
  for (int i = 0; i < NT; i += 2) {
    wait_tail<2, NS - 2>(NT - 2 - i);
    bar();
    if (i + NS < NT) issue();
    rd(G);
    __builtin_amdgcn_sched_barrier(0);
    mm(F);
    if (i < NT - 2) {
      wait_tail<2, NS - 2>(NT - 3 - i);
      bar();
      if (i + 1 + NS < NT) issue();
      rd(F);
    }
    __builtin_amdgcn_sched_barrier(0);
    mm(G);
  }
  TS(2);
  rv = *(const u32x4*)((const char*)res + (MODE == 0 ? off_rm : off_g8));
  float v[8];
#pragma unroll
  for (int i = 0; i < 4; ++i) {
    auto r = __builtin_amdgcn_permlane16_swap(f2u(acc0[i]), f2u(acc1[i]), false, false);
    v[i] = u2f(r[0]);
    v[4 + i] = u2f(r[1]);
  }
  u32x4 ov;
#pragma unroll
  for (int i = 0; i < 4; ++i) ov[i] = pack2(v[2 * i] + bflo(rv[i]), v[2 * i + 1] + bfhi(rv[i]));
  *(u32x4*)((char*)out + (MODE == 0 ? off_g8 : off_rm)) = ov;
  TSE(2 + 2 * MODE);
}

#define K4_NS 6
struct K4F { u32x4 A0, G0, A1, U0, C0, G1, C1, U1; };
__global__ __launch_bounds__(512, 2) void k4_gateup(const u16* __restrict__ h, const u16* __restrict__ wg,
                                                    const u16* __restrict__ wu, u16* __restrict__ act) {
  __shared__ __attribute__((aligned(16))) char smem_[K4_NS * 24576 + 4096];
  lds_char* smem = (lds_char*)smem_;
  const int l = threadIdx.x & 63, wv = __builtin_amdgcn_readfirstlane(threadIdx.x >> 6);
  const int xcd = blockIdx.x & 7, j = blockIdx.x >> 3;
  TSB;
  const int rb = (xcd >> 1) * 4 + (j & 3), cb = (xcd & 1) * 7 + (j >> 2);
  const char* sa = (const char*)h + ((size_t)(wv >> 1) * SEQ + rb * 128 + (wv & 1) * 64) * 16;
  const int lr = wv * 4 + (l >> 4);
  const unsigned gb = lr * (FF * 2) + (((l & 15) ^ (4 * (lr & 3))) << 4);
  const char* sg = (const char*)wg + cb * 256;
  const char* su = (const char*)wu + cb * 256;
  const unsigned lbase = (unsigned)(uintptr_t)smem + wv * 1024;
  int kt = (j * 16) / 28;
  int slot = 0;
  auto issue = [&]() {
    unsigned d = lbase + slot * 24576;
    glds16(sa + (size_t)kt * (4 * SEQ * 16), l * 16, d);
    glds16(sg + (size_t)kt * (32 * FF * 2), gb, d + 8192);
    glds16(su + (size_t)kt * (32 * FF * 2), gb, d + 16384);
    kt = (kt + 1) & 15;
    slot = slot + 1 == K4_NS ? 0 : slot + 1;
  };
#pragma unroll
  for (int s = 0; s < K4_NS; ++s) issue();
  const int rh = wv & 1, cg = wv >> 1;
  const int ksf = rh ^ (cg >> 1), tf = cg & 1;
  const int hb = l >> 5, r32 = l & 31;
  const int a00 = (ksf * 2 + hb) * 2048 + (rh * 64 + tf * 32 + r32) * 16;
  const int a01 = a00 ^ 512, a10 = a00 ^ 4096, a11 = a00 ^ 4096 ^ 512;
  const int q4 = (l & 15) >> 2, p4 = l & 3;
  const int b0 = 8192 + (ksf * 16 + hb * 8 + q4) * 256 + (((cg * 4 + ((l >> 4) & 1) * 2 + (p4 >> 1)) ^ (4 * q4)) << 4) + (p4 & 1) * 8;
  const int b1 = b0 ^ 4096;
  f32x16 ag0 = {}, ag1 = {}, au0 = {}, au1 = {};
  float s1 = 0.f, s2 = 0.f, csg = 0.f, csu = 0.f;
  int cur = 0;
  auto rd = [&](K4F& f) {
    const lds_char* sb = smem + cur * 24576;
    cur = cur + 1 == K4_NS ? 0 : cur + 1;
    f.A0 = lds128(sb + a00); f.G0 = lds_tr2(sb + b0, 1024); f.A1 = lds128(sb + a01); f.U0 = lds_tr2(sb + b0 + 8192, 1024);
    f.C0 = lds128(sb + a10); f.G1 = lds_tr2(sb + b1, 1024); f.C1 = lds128(sb + a11); f.U1 = lds_tr2(sb + b1 + 8192, 1024);
  };
  auto mm = [&](const K4F& f) {
    ag0 = mfma32(f.G0, f.A0, ag0);
    s1 = sum8(f.A0, s1);
    ag1 = mfma32(f.G0, f.A1, ag1);
    s2 = sq8(f.A0, s2);
    au0 = mfma32(f.U0, f.A0, au0);
    csg = sum8(f.G0, csg);
    au1 = mfma32(f.U0, f.A1, au1);
    csu = sum8(f.U0, csu);
    ag0 = mfma32(f.G1, f.C0, ag0);
    ag1 = mfma32(f.G1, f.C1, ag1);
    au0 = mfma32(f.U1, f.C0, au0);
    au1 = mfma32(f.U1, f.C1, au1);
  };
  K4F F, G;
  wait_vm<3 * (K4_NS - 1)>();
  bar();
  TS(1);
  rd(F);
#pragma unroll 1
  for (int i = 0; i < 16; i += 2) {
    wait_tail<3, K4_NS - 2>(14 - i);
    bar();
    if (i + K4_NS < 16) issue();
    rd(G);
    __builtin_amdgcn_sched_barrier(0);
    mm(F);
    if (i < 14) {
      wait_tail<3, K4_NS - 2>(13 - i);
      bar();
      if (i + 1 + K4_NS < 16) issue();
      rd(F);
    }
    __builtin_amdgcn_sched_barrier(0);
    mm(G);
  }
  TS(2);
  lds_char* st = smem + K4_NS * 24576;
  s1 = xsum32(s1); s2 = xsum32(s2); csg = xsum32(csg); csu = xsum32(csu);
  if (hb == 0) LDSF2(st + ((rh * 64 + tf * 32 + r32) * 2 + (cg >> 1)) * 8) = f32x2{s1, s2};
  LDSF(st + 2048 + (((cg * 2 + hb) * 2 + rh) * 32 + r32) * 4) = hb ? csu : csg;
  bar();
  const lds_char* cp = st + 2048 + cg * 512 + hb * 16;
  char* ao = (char*)act + ((size_t)(cb * 16 + cg * 4 + hb) * SEQ + rb * 128 + rh * 64 + r32) * 16;
#pragma unroll
  for (int t = 0; t < 2; ++t) {
    const int tt = t ^ tf;
    f32x4 p = LDSF4(st + (rh * 64 + tt * 32 + r32) * 16);
    float m = (p[0] + p[2]) * (1.f / DM);
    float var = fmaxf((p[1] + p[3]) * (1.f / DM) - m * m, 0.f);
    float r = __builtin_amdgcn_rsqf(var + 1e-5f);
    float mr = -m * r;
    unsigned pk[8];
#pragma unroll
    for (int c = 0; c < 4; ++c) {
      f32x4 cgv = LDSF4(cp + c * 32) + LDSF4(cp + 128 + c * 32);
      f32x4 cuv = LDSF4(cp + 256 + c * 32) + LDSF4(cp + 384 + c * 32);
      float y[4];
#pragma unroll
      for (int e = 0; e < 4; ++e) {
        float gv = (t ? ag1[c * 4 + e] : ag0[c * 4 + e]) * r + cgv[e] * mr;
        float uv = (t ? au1[c * 4 + e] : au0[c * 4 + e]) * r + cuv[e] * mr;
        float sg_ = __builtin_amdgcn_rcpf(1.f + __builtin_amdgcn_exp2f(gv * -1.44269504088896340736f));
        y[e] = gv * sg_ * uv;
      }
      pk[c * 2] = pack2(y[0], y[1]);
      pk[c * 2 + 1] = pack2(y[2], y[3]);
    }
    *(u32x4*)(ao + tt * 512) = u32x4{pk[0], pk[1], pk[2], pk[3]};
    *(u32x4*)(ao + tt * 512 + 2 * SEQ * 16) = u32x4{pk[4], pk[5], pk[6], pk[7]};
  }
  TSE(3);
}

extern "C" void kernel_launch(void* const* d_in, const int* in_sizes, int n_in, void* d_out, int out_size,
                              void* d_ws, size_t ws_size, hipStream_t stream) {
  const u16* x = (const u16*)d_in[0];
  const u16* wqkv = (const u16*)d_in[1];
  const u16* wo = (const u16*)d_in[2];
  const u16* wg = (const u16*)d_in[4];
  const u16* wu = (const u16*)d_in[5];
  const u16* wd = (const u16*)d_in[6];
  char* ws = (char*)d_ws;
  u16* qimg = (u16*)ws;
  u16* kvimg = (u16*)(ws + (2u << 20));
  u16* og = (u16*)(ws + (6u << 20));
  u16* h = (u16*)(ws + (8u << 20));
  u16* act = (u16*)(ws + (10u << 20));
  k1_qkv<<<256, 512, 0, stream>>>(x, wqkv, qimg, kvimg);
  k2_attn<<<256, 512, 0, stream>>>(qimg, kvimg, og);
  k_proj<512, 8, 0><<<256, 512, 0, stream>>>(og, wo, x, h);
  k4_gateup<<<224, 512, 0, stream>>>(h, wg, wu, act);
  k_proj<1792, 9, 1><<<256, 512, 0, stream>>>(act, wd, h, (u16*)d_out);
}
```

```cpp
#include <hip/hip_runtime.h>
#include <hip/hip_bf16.h>
#include <stdint.h>

typedef __attribute__((ext_vector_type(2))) __bf16 bf16x2;
typedef __attribute__((ext_vector_type(8))) __bf16 bf16x8;
typedef __attribute__((ext_vector_type(16))) __bf16 bf16x16;
typedef __attribute__((ext_vector_type(4))) short s16x4;
typedef __attribute__((ext_vector_type(2))) float f32x2;
typedef __attribute__((ext_vector_type(4))) float f32x4;
typedef __attribute__((ext_vector_type(8))) float f32x8;
typedef __attribute__((ext_vector_type(16))) float f32x16;
typedef __attribute__((ext_vector_type(2))) unsigned u32x2;
typedef __attribute__((ext_vector_type(4))) unsigned u32x4;
typedef __attribute__((address_space(3))) char lds_char;
typedef unsigned short u16;

#define DEV __device__ __forceinline__
#define TSB
#define TS(i)
#define TSE(k)
#define SEQ 2048
#define DM 512
#define FF 1792

DEV void glds16(const void* sbase, unsigned voff, unsigned ldsaddr) {
  asm volatile("s_mov_b32 m0, %2\n\ts_nop 0\n\tglobal_load_lds_dwordx4 %0, %1" ::"v"(voff), "s"(sbase), "s"(ldsaddr) : "memory");
}
template <int N> DEV void wait_vm() { asm volatile("s_waitcnt vmcnt(%0)" ::"n"(N) : "memory"); }
template <int P, int D> DEV void wait_tail(int rem) {
  asm volatile(
      "s_waitcnt vmcnt(%1)\n\ts_cmp_ge_i32 %0, %2\n\ts_cbranch_scc1 1f\n\t"
      "s_waitcnt vmcnt(%3)\n\ts_cmp_ge_i32 %0, 3\n\ts_cbranch_scc1 1f\n\t"
      "s_waitcnt vmcnt(%4)\n\ts_cmp_ge_i32 %0, 2\n\ts_cbranch_scc1 1f\n\t"
      "s_waitcnt vmcnt(%5)\n\ts_cmp_ge_i32 %0, 1\n\ts_cbranch_scc1 1f\n\t"
      "s_waitcnt vmcnt(0)\n1:" ::"s"(rem), "n"(P * D), "n"(D), "n"(P * (D < 3 ? D : 3)), "n"(P * (D < 2 ? D : 2)), "n"(P)
      : "memory", "scc");
}
DEV void bar() {
  asm volatile("s_waitcnt lgkmcnt(0)" ::: "memory");
  __builtin_amdgcn_s_barrier();
  asm volatile("" ::: "memory");
}
DEV u32x4 lds128(const lds_char* p) { return *(const __attribute__((address_space(3))) u32x4*)p; }
DEV u32x2 lds_tr(const lds_char* p) {
  s16x4 v = __builtin_amdgcn_ds_read_tr16_b64_v4i16((__attribute__((address_space(3))) s16x4*)p);
  return __builtin_bit_cast(u32x2, v);
}
DEV u32x4 lds_tr2(const lds_char* p, int second) {
  u32x2 a = lds_tr(p), b = lds_tr(p + second);
  return u32x4{a[0], a[1], b[0], b[1]};
}
DEV f32x4 mfma16(u32x4 a, u32x4 b, f32x4 c) {
  return __builtin_amdgcn_mfma_f32_16x16x32_bf16(__builtin_bit_cast(bf16x8, a), __builtin_bit_cast(bf16x8, b), c, 0, 0, 0);
}
DEV f32x16 mfma32(u32x4 a, u32x4 b, f32x16 c) {
  return __builtin_amdgcn_mfma_f32_32x32x16_bf16(__builtin_bit_cast(bf16x8, a), __builtin_bit_cast(bf16x8, b), c, 0, 0, 0);
}
DEV float dot2(unsigned a, unsigned b, float c) {
  return __builtin_amdgcn_fdot2_f32_bf16(__builtin_bit_cast(bf16x2, a), __builtin_bit_cast(bf16x2, b), c, false);
}
DEV float sum8(u32x4 v, float c) {
  c = dot2(v[0], 0x3f803f80u, c); c = dot2(v[1], 0x3f803f80u, c);
  c = dot2(v[2], 0x3f803f80u, c); c = dot2(v[3], 0x3f803f80u, c);
  return c;
}
DEV float sq8(u32x4 v, float c) {
  c = dot2(v[0], v[0], c); c = dot2(v[1], v[1], c); c = dot2(v[2], v[2], c); c = dot2(v[3], v[3], c);
  return c;
}
DEV unsigned pack2(float lo, float hi) {
  bf16x2 v = {(__bf16)lo, (__bf16)hi};
  return __builtin_bit_cast(unsigned, v);
}
DEV float u2f(unsigned u) { return __builtin_bit_cast(float, u); }
DEV unsigned f2u(float f) { return __builtin_bit_cast(unsigned, f); }
DEV float bflo(unsigned u) { return __builtin_bit_cast(float, u << 16); }
DEV float bfhi(unsigned u) { return __builtin_bit_cast(float, u & 0xffff0000u); }
DEV float xsum32(float v) {
  unsigned u = __builtin_bit_cast(unsigned, v);
  auto r = __builtin_amdgcn_permlane32_swap(u, u, false, false);
  return u2f(r[0]) + u2f(r[1]);
}
DEV float xsum16(float v) {
  unsigned u = __builtin_bit_cast(unsigned, v);
  auto r = __builtin_amdgcn_permlane16_swap(u, u, false, false);
  return u2f(r[0]) + u2f(r[1]);
}

DEV void code_prefetch() {
  unsigned long long pc;
  asm volatile("s_getpc_b64 %0" : "=s"(pc));
  pc &= ~255ull;
  unsigned d;
  asm volatile("global_load_dword %0, %1, %2" : "=v"(d) : "v"(threadIdx.x * 32), "s"(pc) : "memory");
}
DEV void touch(const void* base, unsigned off) {
  unsigned d;
  asm volatile("global_load_dword %0, %1, %2" : "=v"(d) : "v"(off), "s"(base) : "memory");
}
#define LDSF(p) (*(__attribute__((address_space(3))) float*)(p))
#define LDSF2(p) (*(__attribute__((address_space(3))) f32x2*)(p))
#define LDSF4(p) (*(__attribute__((address_space(3))) f32x4*)(p))

#define K1_NS 4
struct K1F { u32x4 A0, A1, B0, B1, B2, C0, C1, D0, D1, D2; };
__global__ __launch_bounds__(512, 2) void k1_qkv(const u16* __restrict__ x, const u16* __restrict__ w,
                                                 u16* __restrict__ qimg, u16* __restrict__ kvimg) {
  __shared__ __attribute__((aligned(16))) char smem_[K1_NS * 32768 + 4096];
  lds_char* smem = (lds_char*)smem_;
  const int l = threadIdx.x & 63, wv = __builtin_amdgcn_readfirstlane(threadIdx.x >> 6);
  TSB;
  code_prefetch();
  const int hg = blockIdx.x & 7, rb = blockIdx.x >> 3;
  const int rh = wv & 1, hd = wv >> 1;
  const int lr = wv * 8 + (l >> 3);
  const unsigned ga = lr * (DM * 2) + (((l & 7) ^ ((lr >> 1) & 7)) << 4);
  const unsigned gb = lr * (1536 * 2) + (((l & 7) ^ (2 * (((lr >> 1) & 1) | (((lr >> 3) & 1) << 1)))) << 4);
  const char* xa = (const char*)x + (size_t)rb * (64 * DM * 2);
  const char* wb = (const char*)w + hg * 128;
  const unsigned lbase = (unsigned)(uintptr_t)smem + wv * 1024;
  const int rot = rb & 7;
  auto issue = [&](int st) {
    int kt = (st + rot) & 7;
    unsigned d = lbase + (st % K1_NS) * 32768;
    glds16(xa + kt * 128, ga, d);
    const char* b = wb + (size_t)kt * (64 * 1536 * 2);
    glds16(b, gb, d + 8192);
    glds16(b + 1024, gb, d + 16384);
    glds16(b + 2048, gb, d + 24576);
  };
#pragma unroll
  for (int s = 0; s < K1_NS; ++s) issue(s);

  const int g = l >> 4, c16 = l & 15;
  const int ksf = rh ^ (hd >> 1), tf = hd & 1;
  const int arow = rh * 32 + c16;
  const int a00 = (arow + tf * 16) * 128 + (((ksf * 4 + g) ^ (c16 >> 1)) << 4);
  const int a01 = a00 ^ 2048, a10 = a00 ^ 64, a11 = a00 ^ 2048 ^ 64;
  const int q4 = c16 >> 2, p4 = l & 3;
  const int b0 = 8192 + (ksf * 32 + 8 * g + q4) * 128 + (((hd * 2 + (p4 >> 1)) ^ (2 * ((q4 >> 1) | ((g & 1) << 1)))) << 4) + (p4 & 1) * 8;
  const int b1 = b0 ^ 4096;
  f32x4 acc[3][2] = {};
  float s1 = 0.f, s2 = 0.f, cs[3] = {0.f, 0.f, 0.f};
  auto rd = [&](K1F& f, int st) {
    const lds_char* sb = smem + (st % K1_NS) * 32768;
    f.A0 = lds128(sb + a00); f.B0 = lds_tr2(sb + b0, 512); f.A1 = lds128(sb + a01);
    f.B1 = lds_tr2(sb + b0 + 8192, 512); f.B2 = lds_tr2(sb + b0 + 16384, 512);
    f.C0 = lds128(sb + a10); f.D0 = lds_tr2(sb + b1, 512); f.C1 = lds128(sb + a11);
    f.D1 = lds_tr2(sb + b1 + 8192, 512); f.D2 = lds_tr2(sb + b1 + 16384, 512);
  };
  auto mm = [&](const K1F& f) {
    acc[0][0] = mfma16(f.B0, f.A0, acc[0][0]);
    acc[0][1] = mfma16(f.B0, f.A1, acc[0][1]);
    acc[1][0] = mfma16(f.B1, f.A0, acc[1][0]);
    acc[1][1] = mfma16(f.B1, f.A1, acc[1][1]);
    acc[2][0] = mfma16(f.A0, f.B2, acc[2][0]);
    acc[2][1] = mfma16(f.A1, f.B2, acc[2][1]);
    s1 = sum8(f.A0, s1); s2 = sq8(f.A0, s2);
    cs[0] = sum8(f.B0, cs[0]); cs[1] = sum8(f.B1, cs[1]); cs[2] = sum8(f.B2, cs[2]);
    acc[0][0] = mfma16(f.D0, f.C0, acc[0][0]);
    acc[0][1] = mfma16(f.D0, f.C1, acc[0][1]);
    acc[1][0] = mfma16(f.D1, f.C0, acc[1][0]);
    acc[1][1] = mfma16(f.D1, f.C1, acc[1][1]);
    acc[2][0] = mfma16(f.C0, f.D2, acc[2][0]);
    acc[2][1] = mfma16(f.C1, f.D2, acc[2][1]);
  };
  K1F F, G;
  wait_vm<4 * (K1_NS - 1)>();
  bar();
  TS(1);
  rd(F, 0);
#pragma unroll 1
  for (int i = 0; i < 8; i += 2) {
    wait_tail<4, K1_NS - 2>(6 - i);
    bar();
    if (i + K1_NS < 8) issue(i + K1_NS);
    rd(G, i + 1);
    __builtin_amdgcn_sched_barrier(0);
    mm(F);
    if (i < 6) {
      wait_tail<4, K1_NS - 2>(5 - i);
      bar();
      if (i + 1 + K1_NS < 8) issue(i + 1 + K1_NS);
      rd(F, i + 2);
    }
    __builtin_amdgcn_sched_barrier(0);
    mm(G);
  }
  TS(2);
  lds_char* st = smem + K1_NS * 32768;
  s1 = xsum16(xsum32(s1)); s2 = xsum16(xsum32(s2));
  cs[0] = xsum16(xsum32(cs[0])); cs[1] = xsum16(xsum32(cs[1])); cs[2] = xsum16(xsum32(cs[2]));
  if (g == 0) LDSF2(st + ((arow + tf * 16) * 2 + (hd >> 1)) * 8) = f32x2{s1, s2};
  else LDSF(st + 1024 + (((hd * 3 + g - 1) * 2 + rh) * 16 + c16) * 4) = g == 1 ? cs[0] : (g == 2 ? cs[1] : cs[2]);
  bar();
  lds_char* fin = smem + wv * 256;
  {
    f32x4 p = LDSF4(st + (rh * 32 + (l & 31)) * 16);
    float m = (p[0] + p[2]) * (1.f / DM);
    float var = fmaxf((p[1] + p[3]) * (1.f / DM) - m * m, 0.f);
    float r = __builtin_amdgcn_rsqf(var + 1e-5f);
    if (l < 32) LDSF2(fin + l * 8) = f32x2{r, -m * r};
  }
  const lds_char* cp = st + 1024 + hd * 3 * 128;
  f32x4 cq = LDSF4(cp + g * 16) + LDSF4(cp + 64 + g * 16);
  f32x4 ck = LDSF4(cp + 128 + g * 16) + LDSF4(cp + 192 + g * 16);
  float cv = LDSF(cp + 256 + c16 * 4) + LDSF(cp + 320 + c16 * 4);
  const int head = hg * 4 + hd, t32 = rb * 2 + rh;
  char* qo = (char*)qimg + (size_t)(head * 64 + t32) * 1024 + (g >> 1) * 512 + (g & 1) * 8;
  char* ko = (char*)kvimg + (size_t)(head * 64 + t32) * 2048;
  const float qs = 0.25f * 1.44269504088896340736f;
  u32x2 vp[2];
#pragma unroll
  for (int t = 0; t < 2; ++t) {
    const int tt = t ^ tf;
    f32x2 rm = LDSF2(fin + (tt * 16 + c16) * 8);
    f32x4 qv = (acc[0][t] * rm[0] + cq * rm[1]) * qs;
    f32x4 kv = acc[1][t] * rm[0] + ck * rm[1];
    *(u32x2*)(qo + (tt * 16 + c16) * 16) = u32x2{pack2(qv[0], qv[1]), pack2(qv[2], qv[3])};
    *(u32x2*)(ko + (g >> 1) * 512 + (g & 1) * 8 + (8 * q4 + 4 * tt + p4) * 16) = u32x2{pack2(kv[0], kv[1]), pack2(kv[2], kv[3])};
    f32x4 r01 = LDSF4(fin + (tt * 16 + 4 * g) * 8), r23 = LDSF4(fin + (tt * 16 + 4 * g) * 8 + 16);
    vp[t] = u32x2{pack2(acc[2][t][0] * r01[0] + cv * r01[1], acc[2][t][1] * r01[2] + cv * r01[3]),
                  pack2(acc[2][t][2] * r23[0] + cv * r23[1], acc[2][t][3] * r23[2] + cv * r23[3])};
  }
  u32x4 vv = tf ? u32x4{vp[1][0], vp[1][1], vp[0][0], vp[0][1]} : u32x4{vp[0][0], vp[0][1], vp[1][0], vp[1][1]};
  *(u32x4*)(ko + 1024 + l * 16) = vv;
  TSE(0);
}

DEV f32x16 pv(u32x4 v, f32x8 p, f32x16 o) {
  return __builtin_amdgcn_smfmac_f32_32x32x32_bf16(__builtin_bit_cast(bf16x8, v), __builtin_bit_cast(bf16x16, p), o, 0xdddd, 0, 0);
}
__global__ __launch_bounds__(512, 2) void k2_attn(const u16* __restrict__ qimg, const u16* __restrict__ kvimg,
                                                  u16* __restrict__ og) {
  __shared__ __attribute__((aligned(16))) char smem_[131072 + 4096 + 8192];
  lds_char* smem = (lds_char*)smem_;
  const int l = threadIdx.x & 63, wv = __builtin_amdgcn_readfirstlane(threadIdx.x >> 6);
  const int head = (blockIdx.x & 7) * 4 + ((blockIdx.x >> 3) & 3), qb = blockIdx.x >> 5;
  TSB;
  const char* gk = (const char*)kvimg + (size_t)head * 131072 + wv * 1024;
  const unsigned lbase = (unsigned)(uintptr_t)smem + wv * 1024;
  glds16((const char*)qimg + (size_t)(head * 64 + qb * 8 + wv) * 1024, l * 16, lbase + 135168);
#pragma unroll
  for (int s = 0; s < 16; ++s) {
    glds16(gk + s * 8192, l * 16, lbase + s * 8192);
    if (s == 1 || s == 4) __builtin_amdgcn_s_barrier();
  }
  *(__attribute__((address_space(3))) u32x2*)(smem + 131072 + threadIdx.x * 8) = u32x2{0x3f803f80u, 0x3f803f80u};
  const bool isv = (l & 16) == 0;
  const int va0 = isv ? 1024 + (l >> 5) * 256 + (l & 15) * 16 : 131072 + (l & 15) * 16;
  const int vstep = isv ? 4096 : 0;
  f32x16 cinit = {};
  f32x16 o;
  wait_vm<14>();
  bar();
  TS(1);
  const u32x4 q = lds128(smem + 135168 + wv * 1024 + l * 16);
#pragma unroll 1
  for (int attempt = 0; attempt < 2; ++attempt) {
    o = f32x16{};
    int ka = (unsigned)(uintptr_t)smem + l * 16, va = (unsigned)(uintptr_t)smem + va0;
    int cnt = attempt ? 32 : 3, seg = attempt ? 2 : 0;
#define EX(r) "v_exp_f32 v" #r ", v" #r "\n\t"
#define PV(V, P) "s_waitcnt lgkmcnt(2)\n\tv_smfmac_f32_32x32x32_bf16 v[16:31], " V ", " P ", %[idx]\n\t"
#define QK(S) "s_waitcnt lgkmcnt(2)\n\tv_mfma_f32_32x32x16_bf16 " S ", v[84:87], v[80:83], v[32:47]\n\t"
    asm volatile(
        "ds_read_b128 v[84:87], %[ka]\n\t"
        "v_mov_b32 v72, 0\n\tv_mov_b32 v73, 0\n\tv_mov_b32 v74, 0\n\tv_mov_b32 v75, 0\n\t"
        "v_mov_b32 v76, 0\n\tv_mov_b32 v77, 0\n\tv_mov_b32 v78, 0\n\tv_mov_b32 v79, 0\n\t"
        "v_mov_b32 v92, 0\n\tv_mov_b32 v93, 0\n\tv_mov_b32 v94, 0\n\tv_mov_b32 v95, 0\n\t"
        "s_waitcnt lgkmcnt(0)\n\t"
        "v_mfma_f32_32x32x16_bf16 v[48:63], v[84:87], v[80:83], v[32:47]\n\t"
        "ds_read_b128 v[84:87], %[ka] offset:2048\n\t"
        "ds_read_b128 v[88:91], %[va]\n\t"
        "s_nop 10\n"
        "1:\n\t"
        EX(48) EX(49) PV("v[92:95]", "v[72:79]")
        "ds_read_b128 v[92:95], %[va] offset:512\n\t"
        EX(50) EX(51) EX(52) EX(53) EX(54) QK("v[64:79]")
        "ds_read_b128 v[84:87], %[ka] offset:4096\n\t"
        EX(55) EX(56) EX(57) EX(58) EX(59) EX(60) PV("v[88:91]", "v[48:55]")
        "ds_read_b128 v[88:91], %[va] offset:2048\n\t"
        EX(61) EX(62) EX(63)
        EX(64) EX(65) PV("v[92:95]", "v[56:63]")
        "ds_read_b128 v[92:95], %[va] offset:2560\n\t"
        "v_add_u32 %[va], %[va], %[vs]\n\t"
        EX(66) EX(67) EX(68) EX(69) EX(70) QK("v[48:63]")
        "ds_read_b128 v[84:87], %[ka] offset:6144\n\t"
        "v_add_u32 %[ka], 0x1000, %[ka]\n\t"
        EX(71) EX(72) EX(73) EX(74) EX(75) EX(76) PV("v[88:91]", "v[64:71]")
        "ds_read_b128 v[88:91], %[va]\n\t"
        "s_sub_u32 %[cnt], %[cnt], 1\n\t"
        "s_cmp_lg_u32 %[cnt], 0\n\t"
        EX(77) EX(78) EX(79)
        "s_cbranch_scc1 1b\n\t"
        "s_cmp_eq_u32 %[seg], 2\n\t"
        "s_cbranch_scc1 2f\n\t"
        "s_waitcnt vmcnt(11)\n\t"
        "s_mov_b32 %[cnt], 6\n\t"
        "s_add_u32 %[seg], %[seg], 1\n\t"
        "s_cmp_eq_u32 %[seg], 1\n\t"
        "s_cbranch_scc1 3f\n\t"
        "s_waitcnt vmcnt(0)\n\t"
        "s_mov_b32 %[cnt], 23\n"
        "3:\n\t"
        "s_barrier\n\t"
        "s_branch 1b\n"
        "2:\n\t"
        "s_waitcnt lgkmcnt(0)\n\t"
        "v_smfmac_f32_32x32x32_bf16 v[16:31], v[92:95], v[72:79], %[idx]\n\t"
        "s_nop 15\n\t"
        : "+{v[16:31]}"(o), [ka] "+v"(ka), [va] "+v"(va), [cnt] "+s"(cnt), [seg] "+s"(seg)
        : "{v[32:47]}"(cinit), "{v[80:83]}"(q), [vs] "v"(vstep), [idx] "v"(0xdddd)
        : "memory", "scc", "v48", "v49", "v50", "v51", "v52", "v53", "v54", "v55", "v56", "v57", "v58", "v59", "v60", "v61",
          "v62", "v63", "v64", "v65", "v66", "v67", "v68", "v69", "v70", "v71", "v72", "v73", "v74", "v75", "v76", "v77",
          "v78", "v79", "v84", "v85", "v86", "v87", "v88", "v89", "v90", "v91", "v92", "v93", "v94", "v95");
    if (attempt) break;
    float am = 0.f;
#pragma unroll
    for (int i = 0; i < 8; ++i) am = fmaxf(am, fabsf(o[i]));
    bool ok = o[8] >= 0x1p-60f && o[8] <= 0x1p100f && am <= 0x1p126f;
#pragma unroll
    for (int i = 0; i < 8; ++i) ok = ok && (o[i] == o[i]);
    if (__builtin_amdgcn_ballot_w64(!ok) == 0) break;
    float mx = -__builtin_inff();
#pragma unroll 1
    for (int t = 0; t < 64; ++t) {
      f32x16 s = mfma32(lds128(smem + t * 2048 + l * 16), q, f32x16{});
#pragma unroll
      for (int i = 0; i < 16; ++i) mx = fmaxf(mx, s[i]);
    }
    unsigned u = __builtin_bit_cast(unsigned, mx);
    auto r = __builtin_amdgcn_permlane32_swap(u, u, false, false);
    mx = fmaxf(u2f(r[0]), u2f(r[1]));
#pragma unroll
    for (int i = 0; i < 16; ++i) cinit[i] = -mx;
  }
  TS(2);
  const float inv = 1.f / o[8];
  u32x4 ov = {pack2(o[0] * inv, o[1] * inv), pack2(o[2] * inv, o[3] * inv), pack2(o[4] * inv, o[5] * inv), pack2(o[6] * inv, o[7] * inv)};
  *(u32x4*)((char*)og + ((size_t)(head * 2 + (l >> 5)) * SEQ + qb * 256 + wv * 32 + (l & 31)) * 16) = ov;
  TSE(1);
}

struct KPF { u32x4 A0, B0, B1, A1, D0, D1; };
template <int KDIM, int NS, int MODE>
__global__ __launch_bounds__(512, 2) void k_proj(const u16* __restrict__ a, const u16* __restrict__ w,
                                                 const u16* __restrict__ res, u16* __restrict__ out) {
  constexpr int NT = KDIM / 64;
  __shared__ __attribute__((aligned(16))) char smem_[NS * 16384];
  lds_char* smem = (lds_char*)smem_;
  const int l = threadIdx.x & 63, wv = __builtin_amdgcn_readfirstlane(threadIdx.x >> 6);
  const int xcd = blockIdx.x & 7, j = blockIdx.x >> 3;
  TSB;
  const int rb = xcd * 4 + (j & 3), cb = j >> 2;
  const int lr = wv * 8 + (l >> 3);
  const char* sa = (const char*)a + ((size_t)wv * SEQ + rb * 64) * 16;
  const char* sw = (const char*)w + cb * 128;
  const unsigned gb = lr * (DM * 2) + (((l & 7) ^ (2 * ((lr >> 1) & 3))) << 4);
  const unsigned lbase = (unsigned)(uintptr_t)smem + wv * 1024;
  int kt = (j * NT) >> 5;
  int slot = 0;
  auto issue = [&]() {
    unsigned d = lbase + slot * 16384;
    glds16(sa + (size_t)kt * (8 * SEQ * 16), l * 16, d);
    glds16(sw + (size_t)kt * (64 * DM * 2), gb, d + 8192);
    kt = kt + 1 == NT ? 0 : kt + 1;
    slot = slot + 1 == NS ? 0 : slot + 1;
  };
#pragma unroll
  for (int s = 0; s < NS; ++s) issue();
  const int g = l >> 4, c16 = l & 15;
  const int rt = wv & 3, ch = wv >> 2;
  const int tok = rb * 64 + rt * 16 + c16, col = cb * 64 + ch * 32 + (g & 1) * 16 + (g >> 1) * 8;
  const size_t off_rm = ((size_t)tok * DM + col) * 2, off_g8 = ((size_t)(col >> 3) * SEQ + tok) * 16;
  const int a0 = g * 1024 + (rt * 16 + c16) * 16;
  const int q4 = c16 >> 2, p4 = l & 3;
  const int brow = 16 * (g >> 1) + 4 * (g & 1) + q4;
  const int b0 = 8192 + brow * 128 + (((ch * 4 + (p4 >> 1)) ^ (2 * ((brow >> 1) & 3))) << 4) + (p4 & 1) * 8;
  const int b1 = b0 ^ 32;
  f32x4 acc0 = {}, acc1 = {};
  int cur = 0;
  auto rd = [&](KPF& f) {
    const lds_char* sb = smem + cur * 16384;
    cur = cur + 1 == NS ? 0 : cur + 1;
    f.A0 = lds128(sb + a0); f.B0 = lds_tr2(sb + b0, 1024); f.B1 = lds_tr2(sb + b1, 1024);
    f.A1 = lds128(sb + a0 + 4096); f.D0 = lds_tr2(sb + b0 + 4096, 1024); f.D1 = lds_tr2(sb + b1 + 4096, 1024);
  };
  auto mm = [&](const KPF& f) {
    acc0 = mfma16(f.B0, f.A0, acc0);
    acc1 = mfma16(f.B1, f.A0, acc1);
    acc0 = mfma16(f.D0, f.A1, acc0);
    acc1 = mfma16(f.D1, f.A1, acc1);
  };
  KPF F, G;
  u32x4 rv;
  wait_vm<2 * (NS - 1)>();
  bar();
  TS(1);
  rd(F);
#pragma unroll 1
  for (int i = 0; i < NT; i += 2) {
    wait_tail<2, NS - 2>(NT - 2 - i);
    bar();
    if (i + NS < NT) issue();
    rd(G);
    __builtin_amdgcn_sched_barrier(0);
    mm(F);
    if (i < NT - 2) {
      wait_tail<2, NS - 2>(NT - 3 - i);
      bar();
      if (i + 1 + NS < NT) issue();
      rd(F);
    }
    __builtin_amdgcn_sched_barrier(0);
    mm(G);
  }
  TS(2);
  rv = *(const u32x4*)((const char*)res + (MODE == 0 ? off_rm : off_g8));
  float v[8];
#pragma unroll
  for (int i = 0; i < 4; ++i) {
    auto r = __builtin_amdgcn_permlane16_swap(f2u(acc0[i]), f2u(acc1[i]), false, false);
    v[i] = u2f(r[0]);
    v[4 + i] = u2f(r[1]);
  }
  u32x4 ov;
#pragma unroll
  for (int i = 0; i < 4; ++i) ov[i] = pack2(v[2 * i] + bflo(rv[i]), v[2 * i + 1] + bfhi(rv[i]));
  *(u32x4*)((char*)out + (MODE == 0 ? off_g8 : off_rm)) = ov;
  TSE(2 + 2 * MODE);
}

#define K4_NS 6
struct K4F { u32x4 A0, G0, A1, U0, C0, G1, C1, U1; };
__global__ __launch_bounds__(512, 2) void k4_gateup(const u16* __restrict__ h, const u16* __restrict__ wg,
                                                    const u16* __restrict__ wu, u16* __restrict__ act) {
  __shared__ __attribute__((aligned(16))) char smem_[K4_NS * 24576 + 4096];
  lds_char* smem = (lds_char*)smem_;
  const int l = threadIdx.x & 63, wv = __builtin_amdgcn_readfirstlane(threadIdx.x >> 6);
  const int xcd = blockIdx.x & 7, j = blockIdx.x >> 3;
  TSB;
  const int rb = (xcd >> 1) * 4 + (j & 3), cb = (xcd & 1) * 7 + (j >> 2);
  const char* sa = (const char*)h + ((size_t)(wv >> 1) * SEQ + rb * 128 + (wv & 1) * 64) * 16;
  const int lr = wv * 4 + (l >> 4);
  const unsigned gb = lr * (FF * 2) + (((l & 15) ^ (4 * (lr & 3))) << 4);
  const char* sg = (const char*)wg + cb * 256;
  const char* su = (const char*)wu + cb * 256;
  const unsigned lbase = (unsigned)(uintptr_t)smem + wv * 1024;
  int kt = (j * 16) / 28;
  int slot = 0;
  auto issue = [&]() {
    unsigned d = lbase + slot * 24576;
    glds16(sa + (size_t)kt * (4 * SEQ * 16), l * 16, d);
    glds16(sg + (size_t)kt * (32 * FF * 2), gb, d + 8192);
    glds16(su + (size_t)kt * (32 * FF * 2), gb, d + 16384);
    kt = (kt + 1) & 15;
    slot = slot + 1 == K4_NS ? 0 : slot + 1;
  };
#pragma unroll
  for (int s = 0; s < K4_NS; ++s) issue();
  const int rh = wv & 1, cg = wv >> 1;
  const int ksf = rh ^ (cg >> 1), tf = cg & 1;
  const int hb = l >> 5, r32 = l & 31;
  const int a00 = (ksf * 2 + hb) * 2048 + (rh * 64 + tf * 32 + r32) * 16;
  const int a01 = a00 ^ 512, a10 = a00 ^ 4096, a11 = a00 ^ 4096 ^ 512;
  const int q4 = (l & 15) >> 2, p4 = l & 3;
  const int b0 = 8192 + (ksf * 16 + hb * 8 + q4) * 256 + (((cg * 4 + ((l >> 4) & 1) * 2 + (p4 >> 1)) ^ (4 * q4)) << 4) + (p4 & 1) * 8;
  const int b1 = b0 ^ 4096;
  f32x16 ag0 = {}, ag1 = {}, au0 = {}, au1 = {};
  float s1 = 0.f, s2 = 0.f, csg = 0.f, csu = 0.f;
  int cur = 0;
  auto rd = [&](K4F& f) {
    const lds_char* sb = smem + cur * 24576;
    cur = cur + 1 == K4_NS ? 0 : cur + 1;
    f.A0 = lds128(sb + a00); f.G0 = lds_tr2(sb + b0, 1024); f.A1 = lds128(sb + a01); f.U0 = lds_tr2(sb + b0 + 8192, 1024);
    f.C0 = lds128(sb + a10); f.G1 = lds_tr2(sb + b1, 1024); f.C1 = lds128(sb + a11); f.U1 = lds_tr2(sb + b1 + 8192, 1024);
  };
  auto mm = [&](const K4F& f) {
    ag0 = mfma32(f.G0, f.A0, ag0);
    s1 = sum8(f.A0, s1);
    ag1 = mfma32(f.G0, f.A1, ag1);
    s2 = sq8(f.A0, s2);
    au0 = mfma32(f.U0, f.A0, au0);
    csg = sum8(f.G0, csg);
    au1 = mfma32(f.U0, f.A1, au1);
    csu = sum8(f.U0, csu);
    ag0 = mfma32(f.G1, f.C0, ag0);
    ag1 = mfma32(f.G1, f.C1, ag1);
    au0 = mfma32(f.U1, f.C0, au0);
    au1 = mfma32(f.U1, f.C1, au1);
  };
  K4F F, G;
  wait_vm<3 * (K4_NS - 1)>();
  bar();
  TS(1);
  rd(F);
#pragma unroll 1
  for (int i = 0; i < 16; i += 2) {
    wait_tail<3, K4_NS - 2>(14 - i);
    bar();
    if (i + K4_NS < 16) issue();
    rd(G);
    __builtin_amdgcn_sched_barrier(0);
    mm(F);
    if (i < 14) {
      wait_tail<3, K4_NS - 2>(13 - i);
      bar();
      if (i + 1 + K4_NS < 16) issue();
      rd(F);
    }
    __builtin_amdgcn_sched_barrier(0);
    mm(G);
  }
  TS(2);
  lds_char* st = smem + K4_NS * 24576;
  s1 = xsum32(s1); s2 = xsum32(s2); csg = xsum32(csg); csu = xsum32(csu);
  if (hb == 0) LDSF2(st + ((rh * 64 + tf * 32 + r32) * 2 + (cg >> 1)) * 8) = f32x2{s1, s2};
  LDSF(st + 2048 + (((cg * 2 + hb) * 2 + rh) * 32 + r32) * 4) = hb ? csu : csg;
  bar();
  const lds_char* cp = st + 2048 + cg * 512 + hb * 16;
  char* ao = (char*)act + ((size_t)(cb * 16 + cg * 4 + hb) * SEQ + rb * 128 + rh * 64 + r32) * 16;
#pragma unroll
  for (int t = 0; t < 2; ++t) {
    const int tt = t ^ tf;
    f32x4 p = LDSF4(st + (rh * 64 + tt * 32 + r32) * 16);
    float m = (p[0] + p[2]) * (1.f / DM);
    float var = fmaxf((p[1] + p[3]) * (1.f / DM) - m * m, 0.f);
    float r = __builtin_amdgcn_rsqf(var + 1e-5f);
    float mr = -m * r;
    unsigned pk[8];
#pragma unroll
    for (int c = 0; c < 4; ++c) {
      f32x4 cgv = LDSF4(cp + c * 32) + LDSF4(cp + 128 + c * 32);
      f32x4 cuv = LDSF4(cp + 256 + c * 32) + LDSF4(cp + 384 + c * 32);
      float y[4];
#pragma unroll
      for (int e = 0; e < 4; ++e) {
        float gv = (t ? ag1[c * 4 + e] : ag0[c * 4 + e]) * r + cgv[e] * mr;
        float uv = (t ? au1[c * 4 + e] : au0[c * 4 + e]) * r + cuv[e] * mr;
        float sg_ = __builtin_amdgcn_rcpf(1.f + __builtin_amdgcn_exp2f(gv * -1.44269504088896340736f));
        y[e] = gv * sg_ * uv;
      }
      pk[c * 2] = pack2(y[0], y[1]);
      pk[c * 2 + 1] = pack2(y[2], y[3]);
    }
    *(u32x4*)(ao + tt * 512) = u32x4{pk[0], pk[1], pk[2], pk[3]};
    *(u32x4*)(ao + tt * 512 + 2 * SEQ * 16) = u32x4{pk[4], pk[5], pk[6], pk[7]};
  }
  TSE(3);
}

extern "C" void kernel_launch(void* const* d_in, const int* in_sizes, int n_in, void* d_out, int out_size,
                              void* d_ws, size_t ws_size, hipStream_t stream) {
  const u16* x = (const u16*)d_in[0];
  const u16* wqkv = (const u16*)d_in[1];
  const u16* wo = (const u16*)d_in[2];
  const u16* wg = (const u16*)d_in[4];
  const u16* wu = (const u16*)d_in[5];
  const u16* wd = (const u16*)d_in[6];
  char* ws = (char*)d_ws;
  u16* qimg = (u16*)ws;
  u16* kvimg = (u16*)(ws + (2u << 20));
  u16* og = (u16*)(ws + (6u << 20));
  u16* h = (u16*)(ws + (8u << 20));
  u16* act = (u16*)(ws + (10u << 20));
  k1_qkv<<<256, 512, 0, stream>>>(x, wqkv, qimg, kvimg);
  k2_attn<<<256, 512, 0, stream>>>(qimg, kvimg, og);
  k_proj<512, 8, 0><<<256, 512, 0, stream>>>(og, wo, x, h);
  k4_gateup<<<224, 512, 0, stream>>>(h, wg, wu, act);
  k_proj<1792, 9, 1><<<256, 512, 0, stream>>>(act, wd, h, (u16*)d_out);
}
```

```cpp
#include <hip/hip_runtime.h>
#include <hip/hip_bf16.h>
#include <stdint.h>

typedef __attribute__((ext_vector_type(2))) __bf16 bf16x2;
typedef __attribute__((ext_vector_type(8))) __bf16 bf16x8;
typedef __attribute__((ext_vector_type(16))) __bf16 bf16x16;
typedef __attribute__((ext_vector_type(4))) short s16x4;
typedef __attribute__((ext_vector_type(2))) float f32x2;
typedef __attribute__((ext_vector_type(4))) float f32x4;
typedef __attribute__((ext_vector_type(8))) float f32x8;
typedef __attribute__((ext_vector_type(16))) float f32x16;
typedef __attribute__((ext_vector_type(2))) unsigned u32x2;
typedef __attribute__((ext_vector_type(4))) unsigned u32x4;
typedef __attribute__((address_space(3))) char lds_char;
typedef __attribute__((address_space(1))) unsigned gu32;
typedef __amdgpu_buffer_rsrc_t rsrc_t;
typedef unsigned short u16;

#define DEV __device__ __forceinline__
#ifdef __HIP_DEVICE_COMPILE__
#define NOPK __attribute__((target("no-packed-fp32-ops")))
#else
#define NOPK
#endif
#define TSB
#define TS(i)
#define TSE(k)
#define SEQ 2048
#define DM 512
#define FF 1792
#define MB (1u << 20)
#define O_FA 0
#define O_FB 4096
#define O_EP 8192
#define O_Q (1 * MB)
#define O_KV (3 * MB)
#define O_OG (7 * MB)
#define O_H (9 * MB)
#define O_ACT (11 * MB)
#define O_ST (18 * MB)
#define O_W4 (19 * MB)
#define O_W5 (23 * MB)
#define WS_END (25 * MB)
#define SPIN_MAX 131072
#define SL 4
#define D3 16
#define D4 16
#define D5 8

DEV void glds16(const void* sbase, unsigned voff, unsigned ldsaddr) {
  asm volatile("s_mov_b32 m0, %2\n\ts_nop 0\n\tglobal_load_lds_dwordx4 %0, %1" ::"v"(voff), "s"(sbase), "s"(ldsaddr) : "memory");
}
DEV void glds16c(const void* sbase, unsigned voff, unsigned ldsaddr) {
  asm volatile("s_mov_b32 m0, %2\n\ts_nop 0\n\tglobal_load_lds_dwordx4 %0, %1 sc1" ::"v"(voff), "s"(sbase), "s"(ldsaddr) : "memory");
}
template <int N> DEV void wait_vm() { asm volatile("s_waitcnt vmcnt(%0)" ::"n"(N) : "memory"); }
DEV void bar() {
  asm volatile("s_waitcnt lgkmcnt(0)" ::: "memory");
  __builtin_amdgcn_s_barrier();
  asm volatile("" ::: "memory");
}
#define SB() __builtin_amdgcn_sched_barrier(0)
DEV u32x4 lds128(const lds_char* p) { return *(const __attribute__((address_space(3))) u32x4*)p; }
DEV void sts128(lds_char* p, u32x4 v) { *(__attribute__((address_space(3))) u32x4*)p = v; }
DEV u32x2 lds_tr(const lds_char* p) {
  s16x4 v = __builtin_amdgcn_ds_read_tr16_b64_v4i16((__attribute__((address_space(3))) s16x4*)p);
  return __builtin_bit_cast(u32x2, v);
}
DEV u32x4 lds_tr2(const lds_char* p) {
  u32x2 a = lds_tr(p), b = lds_tr(p + 512);
  return u32x4{a[0], a[1], b[0], b[1]};
}
DEV f32x16 mfma32(u32x4 a, u32x4 b, f32x16 c) {
  return __builtin_amdgcn_mfma_f32_32x32x16_bf16(__builtin_bit_cast(bf16x8, a), __builtin_bit_cast(bf16x8, b), c, 0, 0, 0);
}
DEV float dot2(unsigned a, unsigned b, float c) {
  return __builtin_amdgcn_fdot2_f32_bf16(__builtin_bit_cast(bf16x2, a), __builtin_bit_cast(bf16x2, b), c, false);
}
DEV float sum8(u32x4 v, float c) {
  c = dot2(v[0], 0x3f803f80u, c); c = dot2(v[1], 0x3f803f80u, c);
  c = dot2(v[2], 0x3f803f80u, c); c = dot2(v[3], 0x3f803f80u, c);
  return c;
}
DEV float sq8(u32x4 v, float c) {
  c = dot2(v[0], v[0], c); c = dot2(v[1], v[1], c); c = dot2(v[2], v[2], c); c = dot2(v[3], v[3], c);
  return c;
}
DEV unsigned pack2(float lo, float hi) {
  bf16x2 v = {(__bf16)lo, (__bf16)hi};
  return __builtin_bit_cast(unsigned, v);
}
DEV float u2f(unsigned u) { return __builtin_bit_cast(float, u); }
DEV unsigned f2u(float f) { return __builtin_bit_cast(unsigned, f); }
DEV float bflo(unsigned u) { return __builtin_bit_cast(float, u << 16); }
DEV float bfhi(unsigned u) { return __builtin_bit_cast(float, u & 0xffff0000u); }
DEV float fma1(float a, float b, float c) {
  float d;
  asm("v_fma_f32 %0, %1, %2, %3" : "=v"(d) : "v"(a), "v"(b), "v"(c));
  return d;
}
DEV float xsum32(float v) {
  auto r = __builtin_amdgcn_permlane32_swap(f2u(v), f2u(v), false, false);
  return u2f(r[0]) + u2f(r[1]);
}
DEV float xsum16(float v) {
  auto r = __builtin_amdgcn_permlane16_swap(f2u(v), f2u(v), false, false);
  return u2f(r[0]) + u2f(r[1]);
}
template <int CTRL> DEV float dppadd(float v) {
  return v + u2f(__builtin_amdgcn_update_dpp(0u, f2u(v), CTRL, 0xf, 0xf, true));
}
template <int O> DEV u32x4 pack8(const f32x16& a, float s) {
  return u32x4{pack2(a[O] * s, a[O + 1] * s), pack2(a[O + 2] * s, a[O + 3] * s), pack2(a[O + 4] * s, a[O + 5] * s), pack2(a[O + 6] * s, a[O + 7] * s)};
}
template <int O> DEV u32x4 pack8(const f32x16& a) {
  return u32x4{pack2(a[O], a[O + 1]), pack2(a[O + 2], a[O + 3]), pack2(a[O + 4], a[O + 5]), pack2(a[O + 6], a[O + 7])};
}
DEV u32x4 ld1(rsrc_t r, unsigned v, unsigned s) { return __builtin_amdgcn_raw_buffer_load_b128(r, v, s, 16); }
DEV void st1(rsrc_t r, u32x4 d, unsigned v) { __builtin_amdgcn_raw_buffer_store_b128(d, r, v, 0, 16); }
DEV unsigned nomark(unsigned v) { return v < 0xfffffffeu ? v : 0xfffffffeu; }
DEV u32x4 nomark(u32x4 v) { return u32x4{nomark(v[0]), nomark(v[1]), nomark(v[2]), nomark(v[3])}; }
DEV unsigned umax(unsigned a, unsigned b) { return a > b ? a : b; }
DEV unsigned mk(unsigned m, u32x4 v) { return umax(umax(umax(m, v[0]), v[1]), umax(v[2], v[3])); }
DEV bool marked(unsigned m) { return __builtin_amdgcn_ballot_w64(m == 0xffffffffu) != 0; }
template <int N> DEV void ldn(u32x4* v, rsrc_t r, unsigned vo, unsigned so, unsigned st) {
#pragma unroll
  for (int i = 0; i < N; ++i) { v[i] = ld1(r, vo, so + i * st); SB(); }
}
template <int N> DEV void settle(u32x4* v, rsrc_t r, unsigned vo, unsigned so, unsigned st) {
#pragma unroll 1
  for (int spin = 0; spin < SPIN_MAX; ++spin) {
    asm volatile("" ::: "memory");
    unsigned m[N], a = 0;
#pragma unroll
    for (int i = 0; i < N; ++i) { m[i] = mk(0, v[i]); a = umax(a, m[i]); }
    if (!marked(a)) break;
    if (SL) __builtin_amdgcn_s_sleep(SL);
#pragma unroll
    for (int i = 0; i < N; ++i) if (marked(m[i])) v[i] = ld1(r, vo, so + i * st);
  }
}
DEV lds_char* lxor(const lds_char* p, unsigned m) { return (lds_char*)(uintptr_t)((unsigned)(uintptr_t)p ^ m); }
#define LDSF(p) (*(__attribute__((address_space(3))) float*)(p))
#define LDSF4(p) (*(__attribute__((address_space(3))) f32x4*)(p))

DEV unsigned get_tag(const char* ws) {
  unsigned t;
  asm volatile("s_load_dword %0, %1, %2\n\ts_waitcnt lgkmcnt(0)" : "=s"(t) : "s"(ws), "n"(O_EP) : "memory");
  return t * 0x9e3779b1u + 0x7f4a7c15u;
}
struct K1F { u32x4 a0, q0, k0, v0, a1, q1, k1, v1; };
DEV void k1_qkv(const u16* __restrict__ x, const u16* __restrict__ w, const u16* __restrict__ wg, const u16* __restrict__ wu,
                const u16* __restrict__ wd, const char* ws, rsrc_t rs, lds_char* smem, int wv, unsigned& tag, u32x4* pt) {
  const int l = threadIdx.x & 63;
  TSB;
  const int xcd = blockIdx.x & 7, j = blockIdx.x >> 3;
  const int rb = (xcd >> 1) * 8 + (j & 7), hg = (xcd & 1) * 4 + (j >> 3);
  const int rot = j & 7, w4 = wv & 3;
  const unsigned gb = (w4 * 16 + (l >> 3)) * 3072 + (((l & 7) ^ ((l >> 2) & 4)) << 4);
  const char* wb = (const char*)w + hg * 128;
  const unsigned lbase = (unsigned)(uintptr_t)smem + 16384 + w4 * 2048;
  auto issue = [&](int c) __attribute__((always_inline)) {
    const char* b = wb + (size_t)((c + rot) & 7) * (64 * 3072);
    const unsigned d = lbase + (c % 6) * 24576;
#pragma unroll
    for (int i = 0; i < 3; ++i) {
      glds16(b + i * 1024, gb, d + i * 8192);
      glds16(b + i * 1024 + 8 * 3072, gb, d + i * 8192 + 1024);
    }
  };
  const int u0 = blockIdx.x * 4 + w4, u = u0 < 896 ? u0 : u0 - 128;
  lds_char* pd = smem + 16384 + 2 * 24576 + w4 * 2048;
  if (wv >= 4) {
    u32x4 xr[2][8];
    const char* xa = (const char*)x + (size_t)(rb * 64 + w4 * 16 + (l >> 3)) * 1024 + (l & 7) * 16;
#pragma unroll
    for (int c = 0; c < 8; ++c) {
      xr[0][c] = *(const u32x4*)(xa + ((c + rot) & 7) * 128);
      xr[1][c] = *(const u32x4*)(xa + ((c + rot) & 7) * 128 + 8192);
    }
    SB();
    __builtin_amdgcn_s_barrier();
    tag = get_tag(ws);
    SB();
    float s1[2] = {0.f, 0.f}, s2[2] = {0.f, 0.f}, r[2], mr[2];
#pragma unroll
    for (int c = 0; c < 8; ++c)
#pragma unroll
      for (int g = 0; g < 2; ++g) { s1[g] = sum8(xr[g][c], s1[g]); s2[g] = sq8(xr[g][c], s2[g]); }
#pragma unroll
    for (int g = 0; g < 2; ++g) {
      float a = dppadd<0x141>(dppadd<0x4e>(dppadd<0xb1>(s1[g]))), b = dppadd<0x141>(dppadd<0x4e>(dppadd<0xb1>(s2[g])));
      float m = a * (1.f / DM);
      r[g] = __builtin_amdgcn_rsqf(fmaxf(b * (1.f / DM) - m * m, 0.f) + 1e-5f);
      mr[g] = -m * r[g];
    }
    TS(1);
    lds_char* ad = smem + (w4 * 16 + (l >> 3)) * 128 + (((l & 7) ^ (l >> 4)) << 4);
#pragma unroll
    for (int c = 0; c < 8; ++c) {
#pragma unroll
      for (int g = 0; g < 2; ++g) {
        u32x4 o;
#pragma unroll
        for (int i = 0; i < 4; ++i) o[i] = pack2(fma1(bflo(xr[g][c][i]), r[g], mr[g]), fma1(bfhi(xr[g][c][i]), r[g], mr[g]));
        sts128(lxor(ad, g * 64) + (c & 1) * 8192 + g * 1024, o);
      }
      if (c == 5) wait_vm<23>();
      if (c == 6) wait_vm<17>();
      if (c == 7) wait_vm<11>();
      bar();
      if (c < 3) issue(c + 5);
      SB();
      if (c == 3) {
        const unsigned go = (l >> 3) * (FF * 2) + (l & 7) * 16, gd = (l >> 3) * (DM * 2) + (l & 7) * 16;
        const size_t o4 = (size_t)(u & 31) * (16 * FF * 2) + (u >> 5) * 128, o5 = (size_t)(u >> 3) * (16 * DM * 2) + (u & 7) * 128;
        pt[0] = *(const u32x4*)((const char*)wg + o4 + go);
        pt[1] = *(const u32x4*)((const char*)wg + o4 + 8 * FF * 2 + go);
        pt[2] = *(const u32x4*)((const char*)wu + o4 + go);
        pt[3] = *(const u32x4*)((const char*)wu + o4 + 8 * FF * 2 + go);
        pt[4] = *(const u32x4*)((const char*)wd + o5 + gd);
        pt[5] = *(const u32x4*)((const char*)wd + o5 + 8 * DM * 2 + gd);
      }
      if (c == 2) {
        const u32x4 mark = {~0u, ~0u, ~0u, ~0u};
#pragma unroll
        for (int i = 0; i < 4; ++i) st1(rs, mark, O_OG + u0 * 4096 + i * 1024 + l * 16);
        st1(rs, mark, O_ST + u0 * 128 + (l & 7) * 16);
      }
      if (c == 7) {
#pragma unroll
        for (int i = 0; i < 6; ++i) sts128(pd + (i >> 1) * 8192 + (i & 1) * 1024 + l * 16, pt[i]);
        const lds_char* t = pd + ((l >> 5) * 8 + ((l >> 2) & 3)) * 128 + (l & 1) * 16 + ((l >> 1) & 1) * 8;
        const int g16 = (l >> 4) & 1;
#pragma unroll
        for (int i = 0; i < 4; ++i) pt[i] = lds_tr2(t + g16 * 8192 + i * 32);
#pragma unroll
        for (int i = 0; i < 2; ++i) pt[4 + i] = lds_tr2(t + 16384 + g16 * 32 + i * 64);
      }
      SB();
    }
    TSE(0);
    return;
  }
  unsigned pfd = threadIdx.x * 128;
  asm volatile("s_getpc_b64 s[40:41]\n"
               "91:\n\t"
               "v_min_u32 %0, kf_code_end-91b, %0\n\t"
               "global_load_dword %0, %0, s[40:41]\n\t" : "+v"(pfd) :: "s40", "s41", "memory");
  __builtin_amdgcn_s_barrier();
  const int rh = wv & 1, hp = wv >> 1, hb = l >> 5, r32 = l & 31;
  const int q4 = (l >> 2) & 3, p4 = l & 3;
#pragma unroll
  for (int c = 0; c < 5; ++c) issue(c);
  tag = get_tag(ws);
  SB();
  const int a0 = (rh * 32 + r32) * 128 + ((hb ^ ((r32 >> 1) & 7)) << 4);
  const int b0 = 16384 + (hb * 8 + q4) * 128 + (((hp * 4 + ((l >> 4) & 1) * 2 + (p4 & 1)) ^ ((q4 & 2) * 2)) << 4) + (p4 >> 1) * 8;
  f32x16 aq = {}, ak = {}, av = {};
  auto rd = [&](K1F& f, int c, int h) __attribute__((always_inline)) {
    const lds_char* pa = smem + (c & 1) * 8192;
    const lds_char* pb = smem + b0 + (c % 6) * 24576 + h * 4096;
    f.a0 = lds128(pa + (a0 ^ (h * 64))); f.q0 = lds_tr2(pb); f.k0 = lds_tr2(pb + 8192); f.v0 = lds_tr2(pb + 16384);
    f.a1 = lds128(pa + (a0 ^ (h * 64 + 32))); f.q1 = lds_tr2(pb + 2048); f.k1 = lds_tr2(pb + 10240); f.v1 = lds_tr2(pb + 18432);
  };
  auto mm = [&](const K1F& f) __attribute__((always_inline)) {
    aq = mfma32(f.q0, f.a0, aq); ak = mfma32(f.k0, f.a0, ak); av = mfma32(f.a0, f.v0, av);
    aq = mfma32(f.q1, f.a1, aq); ak = mfma32(f.k1, f.a1, ak); av = mfma32(f.a1, f.v1, av);
  };
  auto il = [&]() __attribute__((always_inline)) {
#pragma unroll
    for (int i = 0; i < 6; ++i) {
      __builtin_amdgcn_sched_group_barrier(0x008, 1, 0);
      __builtin_amdgcn_sched_group_barrier(0x100, 3, 0);
    }
    SB();
  };
  K1F F, G;
#pragma unroll
  for (int c = 0; c < 8; ++c) {
    if (c == 0) wait_vm<24>();
    if (c == 1) wait_vm<18>();
    if (c == 2) wait_vm<12>();
    if (c == 3) wait_vm<6>();
    if (c == 4) wait_vm<0>();
    bar();
    if (c == 0) TS(1);
    if (c == 0) asm volatile("" :: "v"(pfd));
    if (c >= 1) TS(2 + c);
    SB();
    rd(F, c, 0);
    if (c) mm(G);
    il();
    rd(G, c, 1);
    mm(F);
    il();

  }
  mm(G);
  TS(2);
  const int head = hg * 4 + hp * 2, t32 = rb * 2 + rh;
  const unsigned qo = O_Q + (head * 64 + t32) * 1024 + l * 16, ko = O_KV + (head * 64 + t32) * 2048;
  const float qs = 0.25f * 1.44269504088896340736f;
  const unsigned kr = ko + hb * 512 + ((r32 & 19) | ((r32 & 4) << 1) | ((r32 & 8) >> 1)) * 16;
  const unsigned vo = ko + ((l >> 4) & 1) * 131072 + 1024 + hb * 256 + (l & 15) * 16;
  st1(rs, pack8<0>(ak), kr);
  st1(rs, pack8<8>(ak), kr + 131072);
  st1(rs, pack8<0>(aq, qs), qo);
  st1(rs, pack8<8>(aq, qs), qo + 65536);
  st1(rs, pack8<0>(av), vo);
  st1(rs, pack8<8>(av), vo + 512);
  wait_vm<0>();
  if (l == 0) __builtin_amdgcn_raw_buffer_store_b32(tag, rs, O_FA + ((hg * 2 + hp) * 64 + t32) * 4, 0, 16);
  TSE(0);
}

DEV f32x16 pv(u32x4 v, f32x8 p, f32x16 o) {
  return __builtin_amdgcn_smfmac_f32_32x32x32_bf16(__builtin_bit_cast(bf16x8, v), __builtin_bit_cast(bf16x16, p), o, 0xdddd, 0, 0);
}
DEV f32x16 attn_safe(rsrc_t rs, unsigned kv, u32x4 q, int l) {
  float mx = -__builtin_inff();
#pragma unroll 1
  for (int t = 0; t < 64; ++t) {
    f32x16 s = mfma32(ld1(rs, l * 16, kv + t * 2048), q, f32x16{});
#pragma unroll
    for (int i = 0; i < 16; ++i) mx = fmaxf(mx, s[i]);
  }
  auto r = __builtin_amdgcn_permlane32_swap(f2u(mx), f2u(mx), false, false);
  mx = fmaxf(u2f(r[0]), u2f(r[1]));
  f32x16 o = {};
  const u32x4 ones = {0x3f803f80u, 0x3f803f80u, 0x3f803f80u, 0x3f803f80u};
#pragma unroll 1
  for (int t = 0; t < 64; ++t) {
    f32x16 s = mfma32(ld1(rs, l * 16, kv + t * 2048), q, f32x16{});
    const unsigned vp = 1024 + (l >> 5) * 256 + (l & 15) * 16;
    u32x4 lo = ld1(rs, vp, kv + t * 2048), hi = ld1(rs, vp + 512, kv + t * 2048);
    if (l & 16) { lo = ones; hi = ones; }
    f32x8 p0, p1;
#pragma unroll
    for (int i = 0; i < 8; ++i) { p0[i] = __builtin_amdgcn_exp2f(s[i] - mx); p1[i] = __builtin_amdgcn_exp2f(s[8 + i] - mx); }
    o = pv(lo, p0, o);
    o = pv(hi, p1, o);
  }
  return o;
}
DEV bool attn_bad(const f32x16& o) {
  float am = fabsf(o[0]);
#pragma unroll
  for (int i = 1; i < 8; ++i) am += fabsf(o[i]);
  return !(o[8] >= 0x1p-60f && o[8] <= 0x1p100f && am <= 0x1p126f);
}
#define L_ONES 131072
#define L_CTR 134144
#define L_STG 135168
#define IT_SW 12
#define IT_CK 9
#define IT_B1 12
#define W1 0
#define RING 6
#define IT_FB 10
DEV int fresh(int v) { asm volatile("" : "+v"(v)); return v; }
DEV void put_part(lds_char* po, int l, const f32x16& c0, const f32x16& c1) {
  l = fresh(l);
  po += (l & 31) * 256 + ((((l >> 5) * 2) ^ (l & 7) ^ ((l & 4) << 1)) << 4);
#pragma unroll
  for (int e = 0; e < 4; ++e) {
    const int pi = (e >> 1) * 4 + (e & 1);
    LDSF4(lxor(po, pi << 4)) = f32x4{c0[e * 4], c0[e * 4 + 1], c0[e * 4 + 2], c0[e * 4 + 3]};
    LDSF4(lxor(po, (pi + 8) << 4)) = f32x4{c1[e * 4], c1[e * 4 + 1], c1[e * 4 + 2], c1[e * 4 + 3]};
  }
}
template <int N> DEV u32x4 get_part(const lds_char* p, int l, u32x4 r, int o0 = 0) {
  l = fresh(l);
  p += (l & 7) * 256 + ((((l >> 3) * 2) ^ (l & 7) ^ ((l & 4) << 1)) << 4);
  const lds_char* q = lxor(p, 16);
  f32x4 s0 = LDSF4(p + o0), s1 = LDSF4(q + o0);
#pragma unroll
  for (int k = 1; k < N; ++k) { s0 += LDSF4(p + k * 16384); s1 += LDSF4(q + k * 16384); }
  return u32x4{pack2(s0[0] + bflo(r[0]), s0[1] + bfhi(r[0])), pack2(s0[2] + bflo(r[1]), s0[3] + bfhi(r[1])),
               pack2(s1[0] + bflo(r[2]), s1[1] + bfhi(r[2])), pack2(s1[2] + bflo(r[3]), s1[3] + bfhi(r[3]))};
}
#define LDSF2(p) (*(__attribute__((address_space(3))) f32x2*)(p))
#define LDSU(p) ((__attribute__((address_space(3))) unsigned*)(p))

DEV void wait8(lds_char* p) {
#pragma unroll 1
  for (int s = 0; s < SPIN_MAX; ++s) {
    if (__hip_atomic_load(LDSU(p), __ATOMIC_RELAXED, __HIP_MEMORY_SCOPE_WORKGROUP) == 8u) break;
    __builtin_amdgcn_s_sleep(2);
  }
}

__global__ __launch_bounds__(512, 2) NOPK void kf(const u16* __restrict__ x, const u16* __restrict__ wqkv, const u16* __restrict__ wo,
                                                  const u16* __restrict__ wg, const u16* __restrict__ wu, const u16* __restrict__ wd,
                                                  char* ws, u16* __restrict__ out) {
  __shared__ __attribute__((aligned(16))) char smem_[163840];
  lds_char* smem = (lds_char*)smem_;
  const unsigned sbase = (unsigned)(uintptr_t)smem;
  const int wv = __builtin_amdgcn_readfirstlane(threadIdx.x >> 6);
  const rsrc_t rs = __builtin_amdgcn_make_buffer_rsrc(ws, 0, WS_END, 0x00020000);
  unsigned tag;
  u32x4 pt[6];
  asm volatile("" ::"s"(out), "s"(x), "s"(wqkv), "s"(wo), "s"(wg), "s"(wu), "s"(wd));
  k1_qkv(x, wqkv, wg, wu, wd, ws, rs, smem, wv, tag, pt);
  const int l = fresh(threadIdx.x & 63);
  const int b = blockIdx.x, xcd = b & 7, j = b >> 3;
  const int w4 = wv & 3, fol = wv >> 2, hb = l >> 5, r32 = l & 31, q4 = (l >> 2) & 3, p4 = l & 3, g16 = (l >> 4) & 1;
  const bool act4 = j < 28;
  TSB;
  const int head = xcd * 4 + (j & 3), qb = j >> 2;
  const char* kvh = ws + O_KV + (size_t)head * 131072;
  const int rb = xcd * 2 + (j & 1), t3 = (j >> 1) & 1, cb = j >> 2;
  const int ftok = rb * 128 + fol * 64 + t3 * 32 + w4 * 8 + (l & 7);
  const int rb4 = (xcd >> 1) * 4 + (j & 3), cg = fol * 56 + ((xcd & 1) * 7 + (j >> 2)) * 4 + w4;
  u32x4 W[32], res;
  f32x16 oa, ob;
  u32x4 qa, qc;
  {
    if (wv == 0) {
#pragma unroll 1
      for (int spin = 0; spin < SPIN_MAX; ++spin) {
        asm volatile("" ::: "memory");
        if (!__builtin_amdgcn_ballot_w64(__builtin_amdgcn_raw_buffer_load_b32(rs, l * 4, O_FA + (head >> 1) * 256, 16) != tag)) break;
        __builtin_amdgcn_s_sleep(4);
      }
    }
    bar();
    TS(3);
    const char* qp = ws + O_Q + (size_t)(head * 64 + qb * 8 + w4 * 2) * 1024;
    asm volatile("global_load_dwordx4 %0, %2, %4 sc1\n\tglobal_load_dwordx4 %1, %3, %4 sc1" : "=&{v[80:83]}"(qa), "=&{v[84:87]}"(qc)
                 : "v"(l * 16 + fol * 1024), "v"(l * 16 + (fol ^ 1) * 1024), "s"(qp) : "memory");
#pragma unroll
    for (int s = 0; s < 4; ++s) glds16c(kvh + wv * 1024 + s * 8192, l * 16, sbase + wv * 1024 + s * 8192);
    const unsigned one = wv < 6 ? 0x3f803f80u : 0u;
    *(__attribute__((address_space(3))) u32x2*)(smem + L_ONES + threadIdx.x * 8) = u32x2{one, one};
    wait_vm<W1>();
    bar();
    if (fol) {
#pragma unroll
      for (int s = 0; s < 24; ++s) glds16c(kvh + 32768 + w4 * 1024 + s * 4096, l * 16, sbase + 32768 + w4 * 1024 + s * 4096);
      const int u0 = b * 4 + w4, u = u0 < 896 ? u0 : u0 - 128;
#pragma unroll
      for (int i = 0; i < 4; ++i) st1(rs, pt[i], O_W4 + (u >> 5) * 131072 + (u & 31) * 1024 + i * 32768 + l * 16);
#pragma unroll
      for (int i = 0; i < 2; ++i) st1(rs, pt[4 + i], O_W5 + ((u & 7) * 112 + (u >> 3)) * 2048 + i * 1024 + l * 16);
    }
    unsigned ka = sbase + fol * 65536 + l * 16;
    unsigned va = sbase + (l & 15) * 16 + (g16 ? L_ONES : fol * 65536 + 1024 + hb * 256);
    const unsigned vs = (g16 ^ 1) * 4096;
    const uint64_t wsa = (uint64_t)ws;
    const u32x4 rsv = {(unsigned)wsa, (unsigned)(wsa >> 32) & 0xffffu, WS_END, 0x00020000u};
    unsigned sw4 = act4 ? O_W4 + cg * 32768 : O_W5 + (56 + wv) * 28672;
    unsigned gb = (l >> 3) * 1024 + (((l & 7) ^ ((l >> 2) & 4)) << 4);
    int cnt = 16, nxt, dummy;
    uint64_t pc;
#define EX(r) "v_exp_f32 v" #r ", v" #r "\n\t"
#define E2(a, b) EX(a) EX(b)
#define E3(a, b, c) EX(a) EX(b) EX(c)
#define PV(O, Vr, P) "v_smfmac_f32_32x32x32_bf16 " O ", " Vr ", " P ", %[idx]\n\t"
#define QK(S, Q) "v_mfma_f32_32x32x16_bf16 " S ", v[88:91], " Q ", 0\n\t"
#define OA "v[16:31]"
#define OB "v[32:47]"
#define VLO "v[92:95]"
#define VHI "v[96:99]"
#define WL(n) "s_waitcnt lgkmcnt(" #n ")\n\t"
#define PHX(vhi, koff, extra)                                                                        \
  E2(48, 49) PV(OB, VHI, "v[72:79]") "ds_read_b128 " VHI ", %[va] offset:" #vhi "\n\t"              \
  E3(50, 51, 52) QK("v[64:79]", "v[84:87]") "ds_read_b128 v[88:91], %[ka] offset:" #koff "\n\t"     \
  E3(53, 54, 55) extra E2(56, 57) WL(2) PV(OA, VLO, "v[48:55]") E3(58, 59, 60) E3(61, 62, 63)
#define PHY(vlo, extra, extra2)                                                                      \
  E2(64, 65) WL(0) PV(OA, VHI, "v[56:63]") E3(66, 67, 68) QK("v[48:63]", "v[80:83]")                \
  E3(69, 70, 71) extra E2(72, 73) PV(OB, VLO, "v[64:71]") "ds_read_b128 " VLO ", %[va] offset:" #vlo "\n\t" \
  E3(74, 75, 76) extra2 E3(77, 78, 79)
#define STR2(x) #x
#define STR(x) STR2(x)
#define NEXT(d) "s_add_u32 s40, s40, " d "\n\ts_addc_u32 s41, s41, 0\n\t"
    asm volatile(
        ".irp r,16,17,18,19,20,21,22,23,24,25,26,27,28,29,30,31,32,33,34,35,36,37,38,39,40,41,42,43,44,45,46,47,72,73,74,75,76,77,78,79,96,97,98,99\n\t"
        "v_mov_b32 v\\r, 0\n\t.endr\n\t"
        "s_getpc_b64 s[40:41]\n"
        "10:\n\t"
        "s_cmp_eq_u32 %[fol], 0\n\t"
        "s_cbranch_scc1 11f\n\t"
        NEXT("23f-10b")
        "s_mov_b32 %[nxt], " STR(IT_FB) "\n\t"
        "s_setprio 1\n\t"
        "v_mov_b32 v107, %[tag]\n\t"
        "s_waitcnt vmcnt(0)\n\t"
        "s_barrier\n\t"
        "buffer_store_dword v107, %[fb], %[rs], 0 offen sc1\n\t"
        "s_branch 12f\n"
        "11:\n\t"
        NEXT("20f-10b")
        "s_mov_b32 %[nxt], " STR(IT_B1) "\n\t"
        "s_setprio 2\n"
        "12:\n\t"
        "ds_read_b128 v[88:91], %[ka]\n\t"
        "ds_read_b128 " VLO ", %[va]\n\t"
        WL(1) QK("v[48:63]", "v[80:83]")
        "s_nop 12\n\t"
        ".p2align 3\n"
        "1:\n\t"
        PHX(512, 2048, "") PHY(2048, "", "")
        PHX(2560, 4096, "v_add_u32 %[ka], 0x1000, %[ka]\n\t")
        PHY(0, "v_add_u32 %[va], %[va], %[vs]\n\t", "s_sub_u32 %[cnt], %[cnt], 1\n\ts_cmp_gt_u32 %[cnt], %[nxt]\n\t")
        "s_cbranch_scc1 1b\n\t"
        "s_setpc_b64 s[40:41]\n"
        "20:\n\t"
        NEXT("27f-20b")
        "s_waitcnt vmcnt(0)\n\t"
        "s_barrier\n\t"
        "s_mov_b32 m0, " STR(O_FB) "\n\t"
        "s_mov_b32 %[nxt], " STR(IT_CK) "\n\t"
        "s_branch 1b\n"
        "27:\n\t"
        "buffer_load_dwordx4 v[108:111], %[l16], %[rs], m0 offen sc1\n\t"
        "s_sub_u32 %[nxt], %[nxt], 1\n\t"
        "s_cmp_eq_u32 %[nxt], -" STR(SPIN_MAX) "\n\t"
        "s_cbranch_scc1 28f\n\t"
        "s_waitcnt vmcnt(0)\n\t"
        ".irp r,108,109,110,111\n\tv_xor_b32 v\\r, %[tag], v\\r\n\t.endr\n\t"
        "v_or3_b32 v108, v108, v109, v110\n\t"
        "v_or_b32 v108, v108, v111\n\t"
        "v_cmp_ne_u32 vcc, 0, v108\n\t"
        "s_cbranch_vccnz 27b\n"
        "28:\n\t"
        "s_waitcnt vmcnt(0)\n\t"
        ".irp r,108,109,110,111\n\tv_mov_b32 v\\r, -1\n\t.endr\n\t"
        "s_mov_b32 m0, %[am]\n\t"
        ".irp f,0,1,2,3,4,5,6\n\t"
        "buffer_store_dwordx4 v[108:111], %[l16], %[rs], m0 offen sc1\n\t"
        "s_add_u32 m0, m0, 0x8000\n\t"
        ".endr\n\t"
        "s_mov_b32 %[nxt], 13\n\t"
        NEXT("25f-27b")
        "25:\n"
        ".irp g,0,1,2,3\n"
        "21:\n\t"
        ".irp e,0,1\n\t"
        ".irp f,0,1,2,3\n\t"
        "buffer_load_dwordx4 v[128+32*\\g+16*\\e+4*\\f:131+32*\\g+16*\\e+4*\\f], %[l16], %[rs], %[sw4] offen offset:1024*\\f sc1\n\t"
        ".endr\n\t"
        "s_add_u32 %[sw4], %[sw4], 0x1000\n\t"
        ".endr\n\t"
        NEXT("22f-21b")
        "s_branch 1b\n"
        "22:\n\t"
        ".endr\n\t"
        "v_lshlrev_b32 %[dm], 3, %[l16]\n\t"
        "global_load_dwordx4 v[108:111], %[xo], %[x]\n\t"
        "buffer_load_dword v105, %[dm], %[rs], %[pf5] offen\n\t"
        "v_add_u32 %[dm], %[pfc], %[dm]\n\t"
        "v_min_u32 %[dm], kf_code_end-22b, %[dm]\n\t"
        "global_load_dword v107, %[dm], s[40:41]\n\t"
        "s_cmp_eq_u32 %[fol], 0\n\t"
        "s_cselect_b32 %[nxt], 16-" STR(IT_SW) ", 0\n\t"
        "s_cselect_b32 %[sw4], 0, 9f-23f\n\t"
        "s_add_u32 %[sw4], %[sw4], 23f-22b\n\t"
        NEXT("%[sw4]")
        "s_branch 1b\n"
        "23:\n\t"
        "s_cmp_eq_u32 %[fol], 0\n\t"
        "s_cbranch_scc0 24f\n\t"
        "s_setprio 0\n\t"
        "s_mov_b32 %[nxt], 0\n\t"
        NEXT("9f-23b+23b-25b")
        "24:\n\t"
        "s_sub_u32 s40, s40, 23b-25b\n\t"
        "s_subb_u32 s41, s41, 0\n\t"
        "s_mov_b32 m0, %[lo]\n\t"
        "s_barrier\n\t"
        ".irp f,0,1,2,3,4,5,6,7\n\t"
        "global_load_lds_dwordx4 %[gb], %[wos]\n\t"
        "s_add_u32 m0, m0, 0x400\n\t"
        "v_add_u32 %[gb], 0x2000, %[gb]\n\t"
        ".endr\n\t"
        "s_branch 1b\n"
        "9:\n\t"
        "s_setprio 0\n\t"
        PV(OB, VHI, "v[72:79]")
        "s_waitcnt vmcnt(0) lgkmcnt(0)\n\t"
        "s_nop 15\n\t"
        : "={v[16:31]}"(oa), "={v[32:47]}"(ob), "+{v[80:83]}"(qa), "+{v[84:87]}"(qc), [ka] "+v"(ka), [va] "+v"(va), [cnt] "+s"(cnt), [nxt] "=&s"(nxt),
          [sw4] "+s"(sw4), [gb] "+v"(gb), [dm] "=&{v104}"(dummy), "=&{s[40:41]}"(pc), "=&{v[108:111]}"(res),
          "={v[128:131]}"(W[0]), "={v[132:135]}"(W[1]), "={v[136:139]}"(W[2]), "={v[140:143]}"(W[3]), "={v[144:147]}"(W[4]), "={v[148:151]}"(W[5]),
          "={v[152:155]}"(W[6]), "={v[156:159]}"(W[7]), "={v[160:163]}"(W[8]), "={v[164:167]}"(W[9]), "={v[168:171]}"(W[10]), "={v[172:175]}"(W[11]),
          "={v[176:179]}"(W[12]), "={v[180:183]}"(W[13]), "={v[184:187]}"(W[14]), "={v[188:191]}"(W[15]), "={v[192:195]}"(W[16]), "={v[196:199]}"(W[17]),
          "={v[200:203]}"(W[18]), "={v[204:207]}"(W[19]), "={v[208:211]}"(W[20]), "={v[212:215]}"(W[21]), "={v[216:219]}"(W[22]), "={v[220:223]}"(W[23]),
          "={v[224:227]}"(W[24]), "={v[228:231]}"(W[25]), "={v[232:235]}"(W[26]), "={v[236:239]}"(W[27]), "={v[240:243]}"(W[28]), "={v[244:247]}"(W[29]),
          "={v[248:251]}"(W[30]), "={v[252:255]}"(W[31])
        : [vs] "{v100}"(vs), [idx] "{v101}"(0xdddd), [xo] "{v102}"(ftok * 1024 + cb * 128 + (l >> 3) * 16), [l16] "{v103}"(l * 16), [rs] "s"(rsv),
          [fol] "s"(fol), [x] "s"(x), [tag] "s"(tag), [fb] "{v106}"(threadIdx.x == 256 ? O_FB + b * 4 : 0x80000000u),
          [am] "s"(O_ACT + (head * 7 * SEQ + qb * 256 + w4 * 64) * 16), [pf5] "s"(O_W5 + (j * 8 + wv) * 7168), [pfc] "s"(w4 * 8192),
          [wos] "s"((const char*)wo + cb * 128 + wv * 65536), [lo] "s"(sbase + (wv + (wv / 6) * 2) * 8192)
        : "memory", "scc", "vcc", "v48", "v49", "v50", "v51", "v52", "v53", "v54", "v55", "v56", "v57", "v58", "v59", "v60", "v61",
          "v62", "v63", "v64", "v65", "v66", "v67", "v68", "v69", "v70", "v71", "v72", "v73", "v74", "v75", "v76", "v77",
          "v78", "v79", "v88", "v89", "v90", "v91", "v92", "v93", "v94", "v95", "v96", "v97", "v98", "v99", "v105", "v107");
  }
  TS(1);
  {
    lds_char* xo = smem + L_STG + wv * 3072 + l * 16;
    LDSF4(xo) = f32x4{ob[0], ob[1], ob[2], ob[3]};
    LDSF4(xo + 1024) = f32x4{ob[4], ob[5], ob[6], ob[7]};
    LDSF(xo + 2048) = ob[8];
    bar();
    xo = smem + L_STG + (wv ^ 4) * 3072 + l * 16;
    f32x4 p0 = LDSF4(xo), p1 = LDSF4(xo + 1024);
    oa[8] += LDSF(xo + 2048);
#pragma unroll
    for (int i = 0; i < 4; ++i) { oa[i] += p0[i]; oa[4 + i] += p1[i]; }
    if (__builtin_amdgcn_ballot_w64(attn_bad(oa))) oa = attn_safe(rs, O_KV + head * 131072, qa, l);
    st1(rs, nomark(pack8<0>(oa, __builtin_amdgcn_rcpf(oa[8]))), O_OG + ((head * 2 + hb) * SEQ + qb * 256 + w4 * 64 + fol * 32 + r32) * 16);
  }
  TS(2);
  const unsigned vo = hb * (SEQ * 16) + r32 * 16;
  f32x16 cs = {};
  auto colsum = [&]() __attribute__((always_inline)) {
    const u32x4 ones = {0x3f803f80u, 0x3f803f80u, 0x3f803f80u, 0x3f803f80u};
    f32x16 c2 = {};
    cs = f32x16{};
#pragma unroll
    for (int k = 0; k < 32; k += 2) { cs = mfma32(W[k], ones, cs); c2 = mfma32(W[k + 1], ones, c2); }
    cs += c2;
    asm volatile("" : "+v"(cs));
  };
  {
    const int rh = wv & 1, kq = wv >> 1;
    u32x4 A[8];
    const unsigned so = O_OG + (kq * 16 * SEQ + rb * 128 + rh * 64 + t3 * 32) * 16;
    const lds_char* pb = smem + (kq + kq / 3) * 16384 + (hb * 8 + q4) * 128 + (((g16 * 2 + (p4 & 1)) ^ ((q4 & 2) * 2)) << 4) + (p4 >> 1) * 8;
    const lds_char* pc = lxor(pb, 64);
    u32x4 F[4][2];
#pragma unroll
    for (int k = 0; k < 4; ++k) { F[k][0] = lds_tr2(pb + k * 2048); F[k][1] = lds_tr2(pc + k * 2048); }
    if (fol) { colsum(); __builtin_amdgcn_s_sleep(D3 - 16); } else __builtin_amdgcn_s_sleep(D3);
    SB();
    ldn<8>(A, rs, vo, so, 2 * SEQ * 16);
    settle<8>(A, rs, vo, so, 2 * SEQ * 16);
    TS(4);
    f32x16 c0 = {}, c1 = {};
#pragma unroll
    for (int k = 0; k < 8; ++k) {
      c0 = mfma32(F[k & 3][0], A[k], c0);
      c1 = mfma32(F[k & 3][1], A[k], c1);
      if (k < 4) { F[k][0] = lds_tr2(pb + (k + 4) * 2048); F[k][1] = lds_tr2(pc + (k + 4) * 2048); }
      SB();
    }
    put_part(smem + (kq ? 65536 + kq * 16384 : 49152) + rh * 8192, l, c0, c1);
    bar();
    res = nomark(get_part<4>(smem + 65536 + fol * 8192 + w4 * 2048, l, res, -16384));
    st1(rs, res, O_H + ((cb * 8 + (l >> 3)) * SEQ + ftok) * 16);
    float a = dppadd<0x128>(xsum16(xsum32(sum8(res, 0.f)))), q = dppadd<0x128>(xsum16(xsum32(sq8(res, 0.f))));
    if (l < 8) __builtin_amdgcn_raw_buffer_store_b64(u32x2{nomark(f2u(a)), nomark(f2u(q))}, rs, O_ST + (cb * SEQ + ftok) * 8, 0, 16);
    bar();
  }
  TS(5);
  const unsigned sw5 = O_W5 + (cb * 8 + wv) * 28672;
  const unsigned sa5 = O_ACT + (wv * 28 * SEQ + rb * 128 + t3 * 32) * 16;
  u32x4 B[14];
  if (act4) {
    {
      u32x4 T[8];
      const unsigned so = O_H + ((wv >> 1) * SEQ + rb4 * 128 + (wv & 1) * 64) * 16;
      if (!fol) { colsum(); __builtin_amdgcn_s_sleep(D4 - 16); } else __builtin_amdgcn_s_sleep(D4);
      SB();
      ldn<8>(T, rs, l * 16, so, 4 * SEQ * 16);
      settle<8>(T, rs, l * 16, so, 4 * SEQ * 16);
      TS(6);
      lds_char* d = smem + wv * 1024 + l * 16;
      if (fol) {
        u32x4 U[16], S[2];
        const int lq = fresh(l);
        const unsigned su = so + 30 * SEQ * 16, ss = O_ST + (rb4 * 128 + w4 * 32) * 8, sv = (lq >> 4) * (SEQ * 8) + (lq & 15) * 16;
        ldn<8>(U, rs, l * 16, su, 2 * SEQ * 16);
#pragma unroll
        for (int i = 0; i < 8; ++i) sts128(d + i * 8192, T[i]);
        SB();
        ldn<8>(U + 8, rs, l * 16, su + 16 * SEQ * 16, 2 * SEQ * 16);
        ldn<2>(S, rs, sv, ss, 4 * SEQ * 8);
        bar();
#pragma unroll
        for (int c = 0; c < 2; ++c) {
          settle<8>(U + c * 8, rs, l * 16, su + c * 16 * SEQ * 16, 2 * SEQ * 16);
#pragma unroll
          for (int i = c * 8; i < c * 8 + 8; ++i) sts128(d + 65536 - 4096 + i * 4096, U[i]);
        }
        settle<2>(S, rs, sv, ss, 4 * SEQ * 8);
        const u32x4 s0 = S[0], s1 = S[1];
        f32x4 o;
#pragma unroll
        for (int t = 0; t < 2; ++t) {
          const float a = xsum32(xsum16(u2f(s0[2 * t]) + u2f(s1[2 * t]))), q = xsum32(xsum16(u2f(s0[2 * t + 1]) + u2f(s1[2 * t + 1])));
          const float m = a * (1.f / DM);
          o[2 * t] = -m;
          o[2 * t + 1] = __builtin_amdgcn_rsqf(fmaxf(q * (1.f / DM) - m * m, 0.f) + 1e-5f);
        }
        LDSF4(smem + L_STG + w4 * 256 + (fresh(l) & 15) * 16) = o;
        bar();
        __builtin_amdgcn_s_setprio(2);
      } else {
#pragma unroll
        for (int i = 0; i < 8; ++i) sts128(d + i * 8192, T[i]);
        bar();
        __builtin_amdgcn_s_setprio(3);
      }
    }
    TS(7);
    {
      const int lp = fresh(l);
      const lds_char* pa = smem + (lp >> 5) * 2048 + (lp & 31) * 16;
      const lds_char* pb = pa + 65536;
      asm volatile("" : "+v"(pa), "+v"(pb));
      f32x16 cc[4] = {};
      u32x4 A[RING];
#pragma unroll
      for (int n = 0; n < RING - 1; ++n) A[n] = lds128(pa + (n >> 2) * 4096 + (n & 3) * 512);
      SB();
#pragma unroll
      for (int n = 0; n < 128; ++n) {
        const int m = n + RING - 1, k = n >> 2;
        if (m == 64 && !fol) __builtin_amdgcn_s_barrier();
        if (m < 128) A[m % RING] = lds128((m < 64 ? pa : pb) + ((m >> 2) & 15) * 4096 + (m & 3) * 512);
        cc[n & 3] = mfma32(W[k], A[n % RING], cc[n & 3]);
        if ((n & 3) == 3 && k < 28) W[k] = ld1(rs, l * 16, sw5 + k * 1024);
        if ((n & 63) == 63 && l == 0) __hip_atomic_fetch_add(LDSU(smem + L_CTR + (n >> 6) * 4), 1u, __ATOMIC_RELAXED, __HIP_MEMORY_SCOPE_WORKGROUP);
        if (n == 63) TS(8);
        SB();
      }
      TS(9);
#pragma unroll
      for (int t = 0; t < 4; ++t) {
        const f32x16& c = cc[t];
        const f32x2 mr = LDSF2(smem + L_STG + (t * 32 + r32) * 8);
        const float rl = mr[1] * -1.44269504088896340736f, ir2 = __builtin_amdgcn_rcpf(mr[1] * mr[1]);
        u32x4 y;
#pragma unroll
        for (int i = 0; i < 4; ++i) {
          const f32x2 gv = __builtin_elementwise_fma(f32x2{cs[2 * i], cs[2 * i + 1]}, f32x2{mr[0], mr[0]}, f32x2{c[2 * i], c[2 * i + 1]});
          const f32x2 uv = __builtin_elementwise_fma(f32x2{cs[8 + 2 * i], cs[9 + 2 * i]}, f32x2{mr[0], mr[0]}, f32x2{c[8 + 2 * i], c[9 + 2 * i]});
          const f32x2 z = gv * rl;
          const f32x2 e = f32x2{__builtin_amdgcn_exp2f(z[0]), __builtin_amdgcn_exp2f(z[1])} * ir2 + ir2;
          const f32x2 sg = {__builtin_amdgcn_rcpf(e[0]), __builtin_amdgcn_rcpf(e[1])};
          const f32x2 v = gv * (uv * sg);
          y[i] = nomark(pack2(v[0], v[1]));
        }
        st1(rs, y, vo + O_ACT + (cg * 2 * SEQ + rb4 * 128 + t * 32) * 16);
        if (t == 1) TS(10);
        SB();
      }
      TS(11);
    }
    __builtin_amdgcn_s_setprio(0);
  } else {
    __builtin_amdgcn_s_sleep(127);
    __builtin_amdgcn_s_sleep(60);
  }
  if (D5) __builtin_amdgcn_s_sleep(D5);
  ldn<14>(B, rs, vo, sa5, 2 * SEQ * 16);
  {
#pragma unroll
    for (int h = 0; h < 2; ++h) {
      f32x16 c0, c1;
      if (h == 0) settle<14>(B, rs, vo, sa5, 2 * SEQ * 16);
#pragma unroll 1
      for (int pass = 0; pass < 2; ++pass) {
        c0 = f32x16{}; c1 = f32x16{};
        unsigned m = 0;
#pragma unroll
        for (int k = 0; k < 14; ++k) {
          c0 = mfma32(W[2 * k], B[k], c0);
          c1 = mfma32(W[2 * k + 1], B[k], c1);
          if (h == 1) m = fresh(mk(m, B[k]));
          if (h == 0) B[k] = ld1(rs, vo, sa5 + 1024 + k * (2 * SEQ * 16));
          SB();
        }
        if (h == 0 || !marked(m)) break;
        settle<14>(B, rs, vo, sa5 + 1024, 2 * SEQ * 16);
      }
      if (h == 0 && act4) wait8(smem + L_CTR + fol * 4);
      put_part(smem + fol * 65536 + (w4 * 2 + h) * 8192, l, c0, c1);
      if (h == 0) TS(12);
    }
    TS(13);
    bar();
    TS(14);
    const int lf = fresh(l);
    *(u32x4*)((char*)out + (size_t)(rb * 128 + fol * 64 + t3 * 32 + w4 * 8) * 1024 + cb * 128 + (unsigned)((lf & 7) * 1024 + (lf >> 3) * 16)) =
        get_part<8>(smem + fol * 8192 + w4 * 2048, l, res);
  }
  if (b == 0 && threadIdx.x == 0) __builtin_amdgcn_raw_buffer_store_b32(tag, rs, O_EP, 0, 16);
  TSE(1);
  asm volatile("kf_code_end:");
}

extern "C" void kernel_launch(void* const* d_in, const int* in_sizes, int n_in, void* d_out, int out_size,
                              void* d_ws, size_t ws_size, hipStream_t stream) {
  kf<<<256, 512, 0, stream>>>((const u16*)d_in[0], (const u16*)d_in[1], (const u16*)d_in[2], (const u16*)d_in[4],
                              (const u16*)d_in[5], (const u16*)d_in[6], (char*)d_ws, (u16*)d_out);
}
```
